```python
import torch

def _softmax(x, axis=-1):
    return torch.softmax(x, dim=axis)
def _gelu(x):
    return torch.nn.functional.gelu(x, approximate="tanh")
def _silu(x):
    return torch.nn.functional.silu(x)
def _sigmoid(x):
    return torch.sigmoid(x)
def _layernorm(x, axis=-1, eps=1e-5):
    m = x.mean(dim=axis, keepdim=True)
    v = x.var(dim=axis, keepdim=True, unbiased=False)
    return (x - m) / torch.sqrt(v + eps)
def _scatter_add(tgt, idx, src, axis=0):
    return tgt.clone().index_add_(axis, idx, src)
def _segment_sum(src, seg, n):
    out = torch.zeros((n,) + src.shape[1:], dtype=src.dtype)
    return out.index_add_(0, seg, src)
def _pad(x, pw):
    flat = tuple(v for pair in reversed(pw) for v in pair)
    return torch.nn.functional.pad(x, flat)

kernel_name = 'standalone__sliding_block_cached__decode'

def setup_inputs():

    _w = (lambda a: (lambda t: t.to(torch.bfloat16) if t.is_floating_point() else t)(torch.as_tensor(a)))
    B, Sq, D = 1, 1, 640
    Hq, Hkv, Sk, Dh = 32, 32, 16384, 128
    assert Hq % Hkv == 0, (Hq, Hkv)
    return {
        "x": _w((lambda *s: torch.randn(*s, dtype=torch.bfloat16) * 0.8)(B, Sq, D)),
        "k_cache": _w((lambda *s: torch.randn(*s, dtype=torch.bfloat16) * 0.8)(B, Hkv, Sk, Dh)),
        "v_cache": _w((lambda *s: torch.randn(*s, dtype=torch.bfloat16) * 0.05)(B, Hkv, Sk, Dh)),
        "wq": _w((lambda *s: torch.randn(*s, dtype=torch.bfloat16) * 0.8)(D, Hq * Dh) * (D ** -0.5)),
        "wo": _w((lambda *s: torch.randn(*s, dtype=torch.bfloat16) * 0.05)(Hq * Dh, D) * ((Hq * Dh) ** -0.5)),
    }

def reference(x, k_cache, v_cache, wq, wo):
    B, Sq, D = x.shape
    Hq, Dh = 32, 128
    Hkv, Sk = k_cache.shape[1], k_cache.shape[2]
    W = min(4096, max(256, Sk // 4))
    rep = Hq // Hkv
    q = (lambda x, *d: x.permute(*d))((x @ wq).reshape(B, Sq, Hq, Dh), 0, 2, 1, 3)
    k = (lambda x, n, axis: torch.repeat_interleave(x, n, dim=axis))(k_cache[:, :, Sk - W:, :], rep, axis=1)
    v = (lambda x, n, axis: torch.repeat_interleave(x, n, dim=axis))(v_cache[:, :, Sk - W:, :], rep, axis=1)
    scores = torch.einsum("bhqd,bhkd->bhqk", q, k) * (Dh ** -0.5)
    p = _softmax(scores, axis=-1)
    o = torch.einsum("bhqk,bhkd->bhqd", p, v)
    return (lambda x, *d: x.permute(*d))(o, 0, 2, 1, 3).reshape(B, Sq, Hq * Dh) @ wo
```

```cpp
#include <hip/hip_runtime.h>
#include <hip/hip_bf16.h>
#include <stdint.h>

#define GAS __attribute__((address_space(1)))
#define LAS __attribute__((address_space(3)))
typedef unsigned v4u __attribute__((ext_vector_type(4)));
typedef unsigned v2u __attribute__((ext_vector_type(2)));
typedef float f32x2 __attribute__((ext_vector_type(2)));
typedef float f32x4 __attribute__((ext_vector_type(4)));
typedef __bf16 bf16x2 __attribute__((ext_vector_type(2)));

namespace {

constexpr int DM = 640, NH = 32, DH = 128, SK = 16384, WIN = 4096;
constexpr int NSPLIT = 8, NWAVE = 8, NTHR = NWAVE * 64;
constexpr int TOK_WG = WIN / NSPLIT;
constexpr int TOK_WAVE = TOK_WG / NWAVE;
constexpr int QROWS_WAVE = DM / NWAVE;
constexpr int QROWS_LANE = QROWS_WAVE / 4;
constexpr int CB = DM / NSPLIT;
constexpr int WS_EPOCH = 0;
constexpr int WS_ML = 4096;
constexpr int WS_O = 16384;
constexpr int WS_H2 = WS_O + NH * NSPLIT * 1024;
constexpr int WS_END = WS_H2 + 160 * 1024;
constexpr unsigned SPIN_CAP = 1u << 18;
constexpr int AUX_SC1 = 16;

#define KV_NT 1

__device__ __forceinline__ float asf(unsigned u) { return __builtin_bit_cast(float, u); }
__device__ __forceinline__ unsigned asu(float f) { return __builtin_bit_cast(unsigned, f); }
__device__ __forceinline__ float dot2(unsigned a, unsigned b, float acc) {
  return __builtin_amdgcn_fdot2_f32_bf16(__builtin_bit_cast(bf16x2, a), __builtin_bit_cast(bf16x2, b), acc, false);
}
template <int CTRL> __device__ __forceinline__ float dpp(float x) {
  return asf((unsigned)__builtin_amdgcn_mov_dpp((int)asu(x), CTRL, 0xf, 0xf, true));
}
template <int CTRL> __device__ __forceinline__ unsigned dppu(unsigned x) {
  return (unsigned)__builtin_amdgcn_mov_dpp((int)x, CTRL, 0xf, 0xf, true);
}
constexpr int XOR1 = 0xB1, XOR2 = 0x4E, XOR7 = 0x141, XOR8 = 0x128;
__device__ __forceinline__ unsigned cvtpk(float lo, float hi) {
  f32x2 v = {lo, hi};
  return __builtin_bit_cast(unsigned, __builtin_convertvector(v, bf16x2));
}
__device__ __forceinline__ float bf16r(float x) {
  return asf(cvtpk(x, x) & 0xffff0000u);
}
__device__ __forceinline__ float swap32_add(float a, float b) {
  auto r = __builtin_amdgcn_permlane32_swap(asu(a), asu(b), false, false);
  return asf(r[0]) + asf(r[1]);
}
__device__ __forceinline__ float swap16_add(float a, float b) {
  auto r = __builtin_amdgcn_permlane16_swap(asu(a), asu(b), false, false);
  return asf(r[0]) + asf(r[1]);
}
__device__ __forceinline__ float swap32_max(float a) {
  auto r = __builtin_amdgcn_permlane32_swap(asu(a), asu(a), false, false);
  return fmaxf(asf(r[0]), asf(r[1]));
}
__device__ __forceinline__ float swap16_max(float a) {
  auto r = __builtin_amdgcn_permlane16_swap(asu(a), asu(a), false, false);
  return fmaxf(asf(r[0]), asf(r[1]));
}
__device__ __forceinline__ v4u ld_stream(const GAS v4u* p) {
  return __builtin_nontemporal_load(p);
}
#define SB() __builtin_amdgcn_sched_barrier(0)
__device__ __forceinline__ void wg_barrier() {
  asm volatile("s_waitcnt lgkmcnt(0)" ::: "memory");
  __builtin_amdgcn_s_barrier();
  asm volatile("" ::: "memory");
}

__global__ void __launch_bounds__(NTHR, 2)
decode_kernel(const unsigned short* __restrict__ x_, const unsigned short* __restrict__ k_,
              const unsigned short* __restrict__ v_, const unsigned short* __restrict__ wq_,
              const unsigned short* __restrict__ wo_, unsigned short* __restrict__ out_, unsigned char* ws_) {
  __shared__ __attribute__((aligned(16))) float lds[3 * NWAVE * DH + 32];
  float* redq = lds;
  float* redo = lds + NWAVE * DH;
  float* redw = lds + 2 * NWAVE * DH;
  float* wmax = lds + 3 * NWAVE * DH;
  float* wsum = wmax + 8;
  unsigned* lepoch = reinterpret_cast<unsigned*>(wmax + 16);

  const int tid = threadIdx.x, lane = tid & 63;
  const int w = __builtin_amdgcn_readfirstlane(tid >> 6);
  const int r = lane >> 4, c = lane & 15;
  const int b = blockIdx.x;
  const int h = (b & 7) * 4 + ((b >> 3) & 3), s = b >> 5;

  const __amdgpu_buffer_rsrc_t rs = __builtin_amdgcn_make_buffer_rsrc((void*)ws_, 0, WS_END, 0x00020000);

  unsigned ep = 0;
  if (tid == 0) ep = __hip_atomic_load((GAS unsigned*)(ws_ + WS_EPOCH), __ATOMIC_RELAXED, __HIP_MEMORY_SCOPE_AGENT);

  const GAS v2u* xp = (const GAS v2u*)(x_ + QROWS_WAVE * w + QROWS_LANE * r);
  v2u xv[5];
#pragma unroll
  for (int i = 0; i < 5; ++i) xv[i] = xp[i];
  const GAS v4u* wqp = (const GAS v4u*)(wq_ + (size_t)(QROWS_WAVE * w + QROWS_LANE * r) * (NH * DH) + h * DH + 8 * c);
  const size_t kvoff = ((size_t)h * SK + (SK - WIN) + s * TOK_WG + w * TOK_WAVE + r) * DH + 8 * c;
  const GAS v4u* kp = (const GAS v4u*)(k_ + kvoff);
  const GAS v4u* vp = (const GAS v4u*)(v_ + kvoff);
  v4u wq[QROWS_LANE], kk[16], vv[16];
  SB();
#pragma unroll
  for (int i = 0; i < QROWS_LANE; ++i) {
    wq[i] = wqp[(size_t)i * (NH * DH / 8)];
    if (i < 16) kk[i] = ld_stream(kp + i * 64);
    SB();
  }
#pragma unroll
  for (int i = 0; i < 8; ++i) vv[i] = ld_stream(vp + i * 64);
  SB();

  float qa[8];
#pragma unroll
  for (int k = 0; k < 8; ++k) qa[k] = 0.f;
#pragma unroll
  for (int j = 0; j < QROWS_LANE / 2; ++j) {
    const unsigned xpair = xv[j >> 1][j & 1];
#pragma unroll
    for (int k = 0; k < 4; ++k) {
      const unsigned a = wq[2 * j][k], bb = wq[2 * j + 1][k];
      qa[2 * k] = dot2(__builtin_amdgcn_perm(bb, a, 0x05040100u), xpair, qa[2 * k]);
      qa[2 * k + 1] = dot2(__builtin_amdgcn_perm(bb, a, 0x07060302u), xpair, qa[2 * k + 1]);
    }
  }
  SB();
#pragma unroll
  for (int i = 8; i < 16; ++i) vv[i] = ld_stream(vp + i * 64);
  const int ch = c < 10 ? c : 9;
  const GAS v4u* wop = (const GAS v4u*)(wo_ + (size_t)(h * DH + 16 * w + 4 * r) * DM + s * CB + 8 * ch);
  v4u wo[4];
#pragma unroll
  for (int j = 0; j < 4; ++j) wo[j] = wop[j * (DM / 8)];
  SB();
  {
    float u[4], t[2];
#pragma unroll
    for (int k = 0; k < 4; ++k) u[k] = swap32_add(qa[k], qa[k + 4]);
#pragma unroll
    for (int k = 0; k < 2; ++k) t[k] = swap16_add(u[k], u[k + 2]);
    *reinterpret_cast<f32x2*>(redq + w * DH + 8 * c + 2 * r) = f32x2{t[0], t[1]};
    if (tid == 0) *lepoch = ep;
  }
  wg_barrier();
  unsigned qp[4];
  {
    f32x4 q0 = {0.f, 0.f, 0.f, 0.f}, q1 = {0.f, 0.f, 0.f, 0.f};
#pragma unroll
    for (int ww = 0; ww < NWAVE; ++ww) {
      q0 += *reinterpret_cast<const f32x4*>(redq + ww * DH + 8 * c);
      q1 += *reinterpret_cast<const f32x4*>(redq + ww * DH + 8 * c + 4);
    }
    qp[0] = cvtpk(q0[0], q0[1]);
    qp[1] = cvtpk(q0[2], q0[3]);
    qp[2] = cvtpk(q1[0], q1[1]);
    qp[3] = cvtpk(q1[2], q1[3]);
  }
  const unsigned tag = *lepoch + 1u;

  float sc[16];
#pragma unroll
  for (int i = 0; i < 16; ++i) {
    float d = 0.f;
#pragma unroll
    for (int k = 0; k < 4; ++k) d = dot2(kk[i][k], qp[k], d);
    sc[i] = d;
  }
#define TR_STEP(HALF, CTRL, BIT)                                  \
  _Pragma("unroll") for (int i = 0; i < (HALF); ++i) {            \
    const float lo_ = sc[i], hi_ = sc[i + (HALF)];                \
    const bool up_ = (c & (BIT)) != 0;                            \
    const float keep_ = up_ ? hi_ : lo_, send_ = up_ ? lo_ : hi_; \
    sc[i] = keep_ + dpp<CTRL>(send_);                             \
  }
  TR_STEP(8, XOR8, 8) TR_STEP(4, XOR7, 4) TR_STEP(2, XOR2, 2) TR_STEP(1, XOR1, 1)
#undef TR_STEP
  const float sv = bf16r(bf16r(sc[0]) * 0.08838834764831845f);
  float mx = sv;
  mx = fmaxf(mx, dpp<XOR1>(mx));
  mx = fmaxf(mx, dpp<XOR2>(mx));
  mx = fmaxf(mx, dpp<XOR7>(mx));
  mx = fmaxf(mx, dpp<XOR8>(mx));
  mx = swap16_max(mx);
  mx = swap32_max(mx);
  if (lane == 0) wmax[w] = mx;
  wg_barrier();
  float M;
  {
    const f32x4 m0 = *reinterpret_cast<const f32x4*>(wmax), m1 = *reinterpret_cast<const f32x4*>(wmax + 4);
    M = fmaxf(fmaxf(fmaxf(m0[0], m0[1]), fmaxf(m0[2], m0[3])), fmaxf(fmaxf(m1[0], m1[1]), fmaxf(m1[2], m1[3])));
  }
  constexpr float LOG2E = 1.4426950408889634f;
  const float p = __builtin_amdgcn_exp2f((sv - M) * LOG2E);
  {
    float l = p;
    l += dpp<XOR1>(l);
    l += dpp<XOR2>(l);
    l += dpp<XOR7>(l);
    l += dpp<XOR8>(l);
    l = swap16_add(l, l);
    l = swap32_add(l, l);
    if (lane == 0) wsum[w] = l;
  }
  float acc[8];
#pragma unroll
  for (int k = 0; k < 8; ++k) acc[k] = 0.f;
#define PV_STEP(I)                                                                  \
  {                                                                                 \
    const float pi_ = dpp<0x150 + (I)>(p);                                          \
    _Pragma("unroll") for (int k = 0; k < 4; ++k) {                                 \
      const unsigned u_ = vv[I][k];                                                 \
      acc[2 * k] = fmaf(pi_, asf(u_ << 16), acc[2 * k]);      \
      acc[2 * k + 1] = fmaf(pi_, asf(u_ & 0xffff0000u), acc[2 * k + 1]); \
    }                                                                               \
  }
  PV_STEP(0) PV_STEP(1) PV_STEP(2) PV_STEP(3) PV_STEP(4) PV_STEP(5) PV_STEP(6) PV_STEP(7)
  PV_STEP(8) PV_STEP(9) PV_STEP(10) PV_STEP(11) PV_STEP(12) PV_STEP(13) PV_STEP(14) PV_STEP(15)
#undef PV_STEP
  {
    float u[4], t[2];
#pragma unroll
    for (int k = 0; k < 4; ++k) u[k] = swap32_add(acc[k], acc[k + 4]);
#pragma unroll
    for (int k = 0; k < 2; ++k) t[k] = swap16_add(u[k], u[k + 2]);
    *reinterpret_cast<f32x2*>(redo + w * DH + 8 * c + 2 * r) = f32x2{t[0], t[1]};
  }
  wg_barrier();
  if (w == 0) {
    f32x2 o = {0.f, 0.f};
#pragma unroll
    for (int ww = 0; ww < NWAVE; ++ww) o += *reinterpret_cast<const f32x2*>(redo + ww * DH + 2 * lane);
    const v4u rec = {asu(o[0]), tag, asu(o[1]), tag};
    __builtin_amdgcn_raw_buffer_store_b128(rec, rs, WS_O + (h * NSPLIT + s) * 1024 + lane * 16, 0, AUX_SC1);
  } else if (w == 1) {
    const f32x4 l0 = *reinterpret_cast<const f32x4*>(wsum), l1 = *reinterpret_cast<const f32x4*>(wsum + 4);
    const float L = ((l0[0] + l0[1]) + (l0[2] + l0[3])) + ((l1[0] + l1[1]) + (l1[2] + l1[3]));
    const v4u rec = {asu(M), tag, asu(L), tag};
    if (lane == 0) __builtin_amdgcn_raw_buffer_store_b128(rec, rs, WS_ML + (h * NSPLIT + s) * 16, 0, AUX_SC1);
  }
  unsigned p01[8], p23[8];
#pragma unroll
  for (int k = 0; k < 4; ++k) {
    p01[2 * k] = __builtin_amdgcn_perm(wo[1][k], wo[0][k], 0x05040100u);
    p01[2 * k + 1] = __builtin_amdgcn_perm(wo[1][k], wo[0][k], 0x07060302u);
    p23[2 * k] = __builtin_amdgcn_perm(wo[3][k], wo[2][k], 0x05040100u);
    p23[2 * k + 1] = __builtin_amdgcn_perm(wo[3][k], wo[2][k], 0x07060302u);
  }
  {
    const int sp = c & 7, pr = c >> 3;
    const int ooff = WS_O + (h * NSPLIT + sp) * 1024 + (16 * w + 4 * r + 2 * pr) * 8;
    const int moff = WS_ML + (h * NSPLIT + sp) * 16;
    v4u ro, rm;
    for (unsigned spins = 0;; ++spins) {
      ro = __builtin_amdgcn_raw_buffer_load_b128(rs, ooff, 0, AUX_SC1);
      rm = __builtin_amdgcn_raw_buffer_load_b128(rs, moff, 0, AUX_SC1);
      const bool ok = (ro[1] == tag) & (ro[3] == tag) & (rm[1] == tag) & (rm[3] == tag);
      if (__all(ok) || spins > SPIN_CAP) break;
      __builtin_amdgcn_s_sleep(1);
    }
    const float Ms = asf(rm[0]);
    float Mh = Ms;
    Mh = fmaxf(Mh, dpp<XOR1>(Mh));
    Mh = fmaxf(Mh, dpp<XOR2>(Mh));
    Mh = fmaxf(Mh, dpp<XOR7>(Mh));
    const float ws = __builtin_amdgcn_exp2f((Ms - Mh) * LOG2E);
    float Lh = ws * asf(rm[2]);
    float o0 = ws * asf(ro[0]), o1 = ws * asf(ro[2]);
    Lh += dpp<XOR1>(Lh); o0 += dpp<XOR1>(o0); o1 += dpp<XOR1>(o1);
    Lh += dpp<XOR2>(Lh); o0 += dpp<XOR2>(o0); o1 += dpp<XOR2>(o1);
    Lh += dpp<XOR7>(Lh); o0 += dpp<XOR7>(o0); o1 += dpp<XOR7>(o1);
    const float inv = 1.0f / Lh;
    const unsigned mine = cvtpk(o0 * inv, o1 * inv);
    const unsigned other = dppu<XOR8>(mine);
    const unsigned o01 = pr ? other : mine, o23 = pr ? mine : other;
    float g8[8];
#pragma unroll
    for (int k = 0; k < 8; ++k) g8[k] = dot2(p23[k], o23, dot2(p01[k], o01, 0.f));
    float u[4], t[2];
#pragma unroll
    for (int k = 0; k < 4; ++k) u[k] = swap32_add(g8[k], g8[k + 4]);
#pragma unroll
    for (int k = 0; k < 2; ++k) t[k] = swap16_add(u[k], u[k + 2]);
    *reinterpret_cast<f32x2*>(redw + w * DH + 8 * c + 2 * r) = f32x2{t[0], t[1]};
  }
  wg_barrier();
  if (w == 0) {
    if (lane < CB / 2) {
      f32x2 o = {0.f, 0.f};
#pragma unroll
      for (int ww = 0; ww < NWAVE; ++ww) o += *reinterpret_cast<const f32x2*>(redw + ww * DH + 2 * lane);
      const v4u rec = {asu(o[0]), tag, asu(o[1]), tag};
      const int cg = s * (CB / 4) + (lane >> 1);
      __builtin_amdgcn_raw_buffer_store_b128(rec, rs, WS_H2 + cg * 1024 + (lane & 1) * 512 + h * 16, 0, AUX_SC1);
    }
  } else if (w == 1 && h < CB / 4) {
    const int cg = s * (CB / 4) + h;
    v4u rr;
    for (unsigned spins = 0;; ++spins) {
      rr = __builtin_amdgcn_raw_buffer_load_b128(rs, WS_H2 + cg * 1024 + lane * 16, 0, AUX_SC1);
      const bool ok = (rr[1] == tag) & (rr[3] == tag);
      if (__all(ok) || spins > SPIN_CAP) break;
      __builtin_amdgcn_s_sleep(1);
    }
    float a0 = asf(rr[0]), a1 = asf(rr[2]);
    a0 += dpp<XOR1>(a0); a1 += dpp<XOR1>(a1);
    a0 += dpp<XOR2>(a0); a1 += dpp<XOR2>(a1);
    a0 += dpp<XOR7>(a0); a1 += dpp<XOR7>(a1);
    a0 += dpp<XOR8>(a0); a1 += dpp<XOR8>(a1);
    a0 = swap16_add(a0, a0);
    a1 = swap16_add(a1, a1);
    if ((lane & 31) == 0) {
      *reinterpret_cast<GAS unsigned*>((GAS unsigned short*)out_ + 4 * cg + 2 * (lane >> 5)) = cvtpk(a0, a1);
    }
    if (cg == 0 && lane == 0)
      __hip_atomic_store((GAS unsigned*)(ws_ + WS_EPOCH), tag, __ATOMIC_RELAXED, __HIP_MEMORY_SCOPE_AGENT);
  }
}

}

extern "C" void kernel_launch(void* const* d_in, const int* in_sizes, int n_in, void* d_out, int out_size, void* d_ws,
                              size_t ws_size, hipStream_t stream) {
  (void)in_sizes; (void)n_in; (void)out_size; (void)ws_size;
  decode_kernel<<<dim3(NH * NSPLIT), dim3(NTHR), 0, stream>>>(
      (const unsigned short*)d_in[0], (const unsigned short*)d_in[1], (const unsigned short*)d_in[2],
      (const unsigned short*)d_in[3], (const unsigned short*)d_in[4], (unsigned short*)d_out, (unsigned char*)d_ws);
}
```

```cpp
#include <hip/hip_runtime.h>
#include <hip/hip_bf16.h>
#include <stdint.h>

#define GAS __attribute__((address_space(1)))
#define LAS __attribute__((address_space(3)))
typedef unsigned v4u __attribute__((ext_vector_type(4)));
typedef unsigned v2u __attribute__((ext_vector_type(2)));
typedef float f32x2 __attribute__((ext_vector_type(2)));
typedef float f32x4 __attribute__((ext_vector_type(4)));
typedef __bf16 bf16x2 __attribute__((ext_vector_type(2)));

namespace {

constexpr int DM = 640, NH = 32, DH = 128, SK = 16384, WIN = 4096;
constexpr int NSPLIT = 8, NWAVE = 8, NTHR = (NWAVE + 1) * 64;
constexpr int TOK_WG = WIN / NSPLIT;
constexpr int TOK_WAVE = TOK_WG / NWAVE;
constexpr int QROWS_WG = DM / NSPLIT;
constexpr int QROWS_LANE = QROWS_WG / 4;
constexpr int CB = DM / NSPLIT;
constexpr int WS_EPOCH = 0;
constexpr int WS_ML = 4096;
constexpr int WS_O = 16384;
constexpr int WS_Q = WS_O + NH * NSPLIT * 1024;
constexpr int WS_H2 = WS_Q + NH * NSPLIT * 1024;
constexpr int WS_END = WS_H2 + 160 * 1024;
constexpr unsigned SPIN_CAP = 1u << 18;
constexpr int AUX_SC1 = 16;

#define KV_NT 1
#define WO_LATE 0
#define TH_X 16
#define TH_Y 16
#define AHEAD 1
#define STR2(x) #x
#define STR(x) STR2(x)
#define POLL_SLEEP 1
#define WO_NT 1
#define MAP_MODE 1

__device__ __forceinline__ float asf(unsigned u) { return __builtin_bit_cast(float, u); }
__device__ __forceinline__ unsigned asu(float f) { return __builtin_bit_cast(unsigned, f); }
__device__ __forceinline__ float dot2(unsigned a, unsigned b, float acc) {
  return __builtin_amdgcn_fdot2_f32_bf16(__builtin_bit_cast(bf16x2, a), __builtin_bit_cast(bf16x2, b), acc, false);
}
template <int CTRL> __device__ __forceinline__ float dpp(float x) {
  return asf((unsigned)__builtin_amdgcn_mov_dpp((int)asu(x), CTRL, 0xf, 0xf, true));
}
template <int CTRL> __device__ __forceinline__ unsigned dppu(unsigned x) {
  return (unsigned)__builtin_amdgcn_mov_dpp((int)x, CTRL, 0xf, 0xf, true);
}
constexpr int XOR1 = 0xB1, XOR2 = 0x4E, XOR7 = 0x141, XOR8 = 0x128;
__device__ __forceinline__ unsigned cvtpk(float lo, float hi) {
  f32x2 v = {lo, hi};
  return __builtin_bit_cast(unsigned, __builtin_convertvector(v, bf16x2));
}
__device__ __forceinline__ float bf16r(float x) { return asf(cvtpk(x, x) & 0xffff0000u); }
__device__ __forceinline__ float swap32_add(float a, float b) {
  auto r = __builtin_amdgcn_permlane32_swap(asu(a), asu(b), false, false);
  return asf(r[0]) + asf(r[1]);
}
__device__ __forceinline__ float swap16_add(float a, float b) {
  auto r = __builtin_amdgcn_permlane16_swap(asu(a), asu(b), false, false);
  return asf(r[0]) + asf(r[1]);
}
__device__ __forceinline__ float swap32_max(float a) {
  auto r = __builtin_amdgcn_permlane32_swap(asu(a), asu(a), false, false);
  return fmaxf(asf(r[0]), asf(r[1]));
}
__device__ __forceinline__ float swap16_max(float a) {
  auto r = __builtin_amdgcn_permlane16_swap(asu(a), asu(a), false, false);
  return fmaxf(asf(r[0]), asf(r[1]));
}
__device__ __forceinline__ v4u ld_stream(const GAS v4u* p) {
  return __builtin_nontemporal_load(p);
}
#define SB() __builtin_amdgcn_sched_barrier(0)
#define STAMP(n)
#define LDS_ST(p, v) __hip_atomic_store((p), (v), __ATOMIC_RELAXED, __HIP_MEMORY_SCOPE_WORKGROUP)
#define LDS_LD(p) __hip_atomic_load((p), __ATOMIC_RELAXED, __HIP_MEMORY_SCOPE_WORKGROUP)
__device__ __forceinline__ void wg_barrier() {
  SB();
  asm volatile("s_waitcnt lgkmcnt(0)" ::: "memory");
  __builtin_amdgcn_s_barrier();
  asm volatile("" ::: "memory");
  SB();
}

__global__ void __launch_bounds__(NTHR, 3)
decode_kernel(const unsigned short* __restrict__ x_, const unsigned short* __restrict__ k_,
              const unsigned short* __restrict__ v_, const unsigned short* __restrict__ wq_,
              const unsigned short* __restrict__ wo_, unsigned short* __restrict__ out_, unsigned char* ws_) {
  __shared__ __attribute__((aligned(16))) float lds[2 * NWAVE * DH + 64 + 32];
  float* redo = lds;
  float* redw = lds + NWAVE * DH;
  unsigned* qs = reinterpret_cast<unsigned*>(lds + 2 * NWAVE * DH);
  float* wmax = lds + 2 * NWAVE * DH + 64;
  float* wsum = wmax + 8;
  LAS unsigned* lflag = (LAS unsigned*)(wmax + 16);
  STAMP(0);
  const int tid = threadIdx.x, lane = tid & 63;
  const int wv = __builtin_amdgcn_readfirstlane(tid >> 6);
  const int w = wv - 1;
  const int r = lane >> 4, c = lane & 15;
  const int b = blockIdx.x;
  const int h = b >> 3, s = b & 7;
  const int h2 = (b & 7) * 4 + ((b >> 3) & 3), c2 = b >> 5;

  const __amdgpu_buffer_rsrc_t rs = __builtin_amdgcn_make_buffer_rsrc((void*)ws_, 0, WS_END, 0x00020000);
  const size_t kvoff = ((size_t)h * SK + (SK - WIN) + s * TOK_WG + w * TOK_WAVE + r) * DH + 8 * c;
  const GAS v4u* kp = (const GAS v4u*)(k_ + kvoff);
  const GAS v4u* vp = (const GAS v4u*)(v_ + kvoff);
  v4u kk[16], vv[16];

  if (wv == 0) {
    __builtin_amdgcn_s_setprio(3);
    const unsigned ep = __hip_atomic_load((GAS unsigned*)(ws_ + WS_EPOCH), __ATOMIC_RELAXED, __HIP_MEMORY_SCOPE_AGENT);
    const GAS v2u* xp = (const GAS v2u*)(x_ + QROWS_WG * s + QROWS_LANE * r);
    v2u xv[5];
#pragma unroll
    for (int i = 0; i < 5; ++i) xv[i] = xp[i];
    SB();
    const GAS v4u* wqp = (const GAS v4u*)(wq_ + (size_t)(QROWS_WG * s + QROWS_LANE * r) * (NH * DH) + h * DH + 8 * c);
    v4u wq[QROWS_LANE];
#pragma unroll
    for (int i = 0; i < QROWS_LANE; ++i) wq[i] = ld_stream(wqp + (size_t)i * (NH * DH / 8));
    SB();
    if (lane == 0) { LDS_ST(lflag, 0u); LDS_ST(lflag + 2, 0u); }
    wg_barrier();
    float qa[8];
#pragma unroll
    for (int k = 0; k < 8; ++k) qa[k] = 0.f;
#pragma unroll
    for (int j = 0; j < QROWS_LANE / 2; ++j) {
      const unsigned xpair = xv[j >> 1][j & 1];
#pragma unroll
      for (int k = 0; k < 4; ++k) {
        const unsigned a = wq[2 * j][k], bb = wq[2 * j + 1][k];
        qa[2 * k] = dot2(__builtin_amdgcn_perm(bb, a, 0x05040100u), xpair, qa[2 * k]);
        qa[2 * k + 1] = dot2(__builtin_amdgcn_perm(bb, a, 0x07060302u), xpair, qa[2 * k + 1]);
      }
    }
    const unsigned tg = ep + 1u;
    float u[4], t[2];
#pragma unroll
    for (int k = 0; k < 4; ++k) u[k] = swap32_add(qa[k], qa[k + 4]);
#pragma unroll
    for (int k = 0; k < 2; ++k) t[k] = swap16_add(u[k], u[k + 2]);
    const v4u rec = {asu(t[0]), tg, asu(t[1]), tg};
    __builtin_amdgcn_raw_buffer_store_b128(rec, rs, WS_Q + (h * NSPLIT + s) * 1024 + (8 * c + 2 * r) * 8, 0, AUX_SC1);
    STAMP(1);
    v4u g[NSPLIT];
    for (unsigned spins = 0;; ++spins) {
      bool ok = true;
#pragma unroll
      for (int j = 0; j < NSPLIT; ++j)
        g[j] = __builtin_amdgcn_raw_buffer_load_b128(rs, WS_Q + (h * NSPLIT + j) * 1024 + lane * 16, 0, AUX_SC1);
#pragma unroll
      for (int j = 0; j < NSPLIT; ++j) ok &= (g[j][1] == tg) & (g[j][3] == tg);
      if (__all(ok) || spins > SPIN_CAP) break;
      __builtin_amdgcn_s_sleep(1);
    }
    float q0 = 0.f, q1 = 0.f;
#pragma unroll
    for (int j = 0; j < NSPLIT; ++j) { q0 += asf(g[j][0]); q1 += asf(g[j][2]); }
    qs[lane] = cvtpk(q0, q1);
    if (lane == 0) LDS_ST(lflag + 1, tg);
    asm volatile("s_waitcnt lgkmcnt(0)" ::: "memory");
    if (lane == 0) LDS_ST(lflag, 1u);
    STAMP(2);
    wg_barrier();
    wg_barrier();
    return;
  }
  wg_barrier();
  if (w >= AHEAD) {
    for (unsigned spins = 0; spins < (SPIN_CAP << 4); ++spins) {
      if ((int)__builtin_amdgcn_readfirstlane(LDS_LD(lflag + 2)) > w - AHEAD) break;
      __builtin_amdgcn_s_sleep(1);
    }
  }
  STAMP(1);
  SB();
#pragma unroll
  for (int i = 0; i < 16; ++i) kk[i] = ld_stream(kp + i * 64);
  SB();
#pragma unroll
  for (int i = 0; i < 16; ++i) vv[i] = ld_stream(vp + i * 64);
  SB();
  asm volatile("s_waitcnt vmcnt(" STR(TH_X) ")" ::: "memory");
  if (lane == 0) __hip_atomic_fetch_add(lflag + 2, 1u, __ATOMIC_RELAXED, __HIP_MEMORY_SCOPE_WORKGROUP);
  SB();
  const int ch = c < 10 ? c : 9;
  const GAS v4u* wop = (const GAS v4u*)(wo_ + (size_t)(h2 * DH + 16 * w + 4 * r) * DM + c2 * CB + 8 * ch);
  v4u wo[4];
#pragma unroll
  for (int j = 0; j < 4; ++j) wo[j] = WO_NT ? __builtin_nontemporal_load(wop + j * (DM / 8)) : wop[j * (DM / 8)];
  SB();
  STAMP(3);
  for (unsigned spins = 0; spins < (SPIN_CAP << 4); ++spins) {
    if (__builtin_amdgcn_readfirstlane(LDS_LD(lflag)) != 0u) break;
    __builtin_amdgcn_s_sleep(2);
  }
  asm volatile("" ::: "memory");
  const v4u qp = *reinterpret_cast<const v4u*>(qs + 4 * c);
  const unsigned tag = LDS_LD(lflag + 1);
  STAMP(4);

  float sc[16];
#pragma unroll
  for (int i = 0; i < 16; ++i) {
    float d = 0.f;
#pragma unroll
    for (int k = 0; k < 4; ++k) d = dot2(kk[i][k], qp[k], d);
    sc[i] = d;
  }
#define TR_STEP(HALF, CTRL, BIT)                                  \
  _Pragma("unroll") for (int i = 0; i < (HALF); ++i) {            \
    const float lo_ = sc[i], hi_ = sc[i + (HALF)];                \
    const bool up_ = (c & (BIT)) != 0;                            \
    const float keep_ = up_ ? hi_ : lo_, send_ = up_ ? lo_ : hi_; \
    sc[i] = keep_ + dpp<CTRL>(send_);                             \
  }
  TR_STEP(8, XOR8, 8) TR_STEP(4, XOR7, 4) TR_STEP(2, XOR2, 2) TR_STEP(1, XOR1, 1)
#undef TR_STEP
  const float sv = bf16r(bf16r(sc[0]) * 0.08838834764831845f);
  float mx = sv;
  mx = fmaxf(mx, dpp<XOR1>(mx));
  mx = fmaxf(mx, dpp<XOR2>(mx));
  mx = fmaxf(mx, dpp<XOR7>(mx));
  mx = fmaxf(mx, dpp<XOR8>(mx));
  mx = swap16_max(mx);
  mx = swap32_max(mx);
  constexpr float LOG2E = 1.4426950408889634f;
  const float p = __builtin_amdgcn_exp2f((sv - mx) * LOG2E);
  {
    float l = p;
    l += dpp<XOR1>(l);
    l += dpp<XOR2>(l);
    l += dpp<XOR7>(l);
    l += dpp<XOR8>(l);
    l = swap16_add(l, l);
    l = swap32_add(l, l);
    if (lane == 0) { wmax[w] = mx; wsum[w] = l; }
  }
  STAMP(5);
  float acc[8];
#pragma unroll
  for (int k = 0; k < 8; ++k) acc[k] = 0.f;
#define PV_STEP(I)                                                     \
  {                                                                    \
    const float pi_ = dpp<0x150 + (I)>(p);                             \
    _Pragma("unroll") for (int k = 0; k < 4; ++k) {                    \
      const unsigned u_ = vv[I][k];                                    \
      acc[2 * k] = fmaf(pi_, asf(u_ << 16), acc[2 * k]);               \
      acc[2 * k + 1] = fmaf(pi_, asf(u_ & 0xffff0000u), acc[2 * k + 1]); \
    }                                                                  \
  }
  PV_STEP(0) PV_STEP(1) PV_STEP(2) PV_STEP(3) PV_STEP(4) PV_STEP(5) PV_STEP(6) PV_STEP(7)
  PV_STEP(8) PV_STEP(9) PV_STEP(10) PV_STEP(11) PV_STEP(12) PV_STEP(13) PV_STEP(14) PV_STEP(15)
#undef PV_STEP
  {
    float u[4], t[2];
#pragma unroll
    for (int k = 0; k < 4; ++k) u[k] = swap32_add(acc[k], acc[k + 4]);
#pragma unroll
    for (int k = 0; k < 2; ++k) t[k] = swap16_add(u[k], u[k + 2]);
    *reinterpret_cast<f32x2*>(redo + w * DH + 8 * c + 2 * r) = f32x2{t[0], t[1]};
  }
  STAMP(6);
  wg_barrier();
  STAMP(7);
  if (w == 0) {
    const f32x4 m0 = *reinterpret_cast<const f32x4*>(wmax), m1 = *reinterpret_cast<const f32x4*>(wmax + 4);
    const f32x4 l0 = *reinterpret_cast<const f32x4*>(wsum), l1 = *reinterpret_cast<const f32x4*>(wsum + 4);
    const float M = fmaxf(fmaxf(fmaxf(m0[0], m0[1]), fmaxf(m0[2], m0[3])), fmaxf(fmaxf(m1[0], m1[1]), fmaxf(m1[2], m1[3])));
    float o0 = 0.f, o1 = 0.f, L = 0.f;
#pragma unroll
    for (int ww = 0; ww < NWAVE; ++ww) {
      const float mw = ww < 4 ? m0[ww & 3] : m1[ww & 3], lw = ww < 4 ? l0[ww & 3] : l1[ww & 3];
      const float f = __builtin_amdgcn_exp2f((mw - M) * LOG2E);
      const f32x2 ov = *reinterpret_cast<const f32x2*>(redo + ww * DH + 2 * lane);
      o0 = fmaf(f, ov[0], o0);
      o1 = fmaf(f, ov[1], o1);
      L = fmaf(f, lw, L);
    }
    const v4u rec = {asu(o0), tag, asu(o1), tag};
    __builtin_amdgcn_raw_buffer_store_b128(rec, rs, WS_O + (h * NSPLIT + s) * 1024 + lane * 16, 0, AUX_SC1);
    const v4u rec2 = {asu(M), tag, asu(L), tag};
    if (lane == 0) __builtin_amdgcn_raw_buffer_store_b128(rec2, rs, WS_ML + (h * NSPLIT + s) * 16, 0, AUX_SC1);
  }
  {
    const int sp = c & 7, pr = c >> 3;
    const int ooff = WS_O + (h2 * NSPLIT + sp) * 1024 + (16 * w + 4 * r + 2 * pr) * 8;
    const int moff = WS_ML + (h2 * NSPLIT + sp) * 16;
    v4u ro = __builtin_amdgcn_raw_buffer_load_b128(rs, ooff, 0, AUX_SC1);
    v4u rm = __builtin_amdgcn_raw_buffer_load_b128(rs, moff, 0, AUX_SC1);
    for (unsigned spins = 0;; ++spins) {
      const bool oko = (ro[1] == tag) & (ro[3] == tag), okm = (rm[1] == tag) & (rm[3] == tag);
      if (__all(oko & okm) || spins > SPIN_CAP) break;
      __builtin_amdgcn_s_sleep(POLL_SLEEP);
      if (!oko) ro = __builtin_amdgcn_raw_buffer_load_b128(rs, ooff, 0, AUX_SC1);
      if (!okm) rm = __builtin_amdgcn_raw_buffer_load_b128(rs, moff, 0, AUX_SC1);
    }
    STAMP(8);
    unsigned p01[8], p23[8];
#pragma unroll
    for (int k = 0; k < 4; ++k) {
      p01[2 * k] = __builtin_amdgcn_perm(wo[1][k], wo[0][k], 0x05040100u);
      p01[2 * k + 1] = __builtin_amdgcn_perm(wo[1][k], wo[0][k], 0x07060302u);
      p23[2 * k] = __builtin_amdgcn_perm(wo[3][k], wo[2][k], 0x05040100u);
      p23[2 * k + 1] = __builtin_amdgcn_perm(wo[3][k], wo[2][k], 0x07060302u);
    }
    const float Ms = asf(rm[0]);
    float Mh = Ms;
    Mh = fmaxf(Mh, dpp<XOR1>(Mh));
    Mh = fmaxf(Mh, dpp<XOR2>(Mh));
    Mh = fmaxf(Mh, dpp<XOR7>(Mh));
    const float ws = __builtin_amdgcn_exp2f((Ms - Mh) * LOG2E);
    float Lh = ws * asf(rm[2]);
    float o0 = ws * asf(ro[0]), o1 = ws * asf(ro[2]);
    Lh += dpp<XOR1>(Lh); o0 += dpp<XOR1>(o0); o1 += dpp<XOR1>(o1);
    Lh += dpp<XOR2>(Lh); o0 += dpp<XOR2>(o0); o1 += dpp<XOR2>(o1);
    Lh += dpp<XOR7>(Lh); o0 += dpp<XOR7>(o0); o1 += dpp<XOR7>(o1);
    const float inv = 1.0f / Lh;
    const unsigned mine = cvtpk(o0 * inv, o1 * inv);
    const unsigned other = dppu<XOR8>(mine);
    const unsigned o01 = pr ? other : mine, o23 = pr ? mine : other;
    float g8[8];
#pragma unroll
    for (int k = 0; k < 8; ++k) g8[k] = dot2(p23[k], o23, dot2(p01[k], o01, 0.f));
    float u[4], t[2];
#pragma unroll
    for (int k = 0; k < 4; ++k) u[k] = swap32_add(g8[k], g8[k + 4]);
#pragma unroll
    for (int k = 0; k < 2; ++k) t[k] = swap16_add(u[k], u[k + 2]);
    *reinterpret_cast<f32x2*>(redw + w * DH + 8 * c + 2 * r) = f32x2{t[0], t[1]};
  }
  wg_barrier();
  STAMP(9);
  if (w == 0) {
    if (lane < CB / 2) {
      f32x2 o = {0.f, 0.f};
#pragma unroll
      for (int ww = 0; ww < NWAVE; ++ww) o += *reinterpret_cast<const f32x2*>(redw + ww * DH + 2 * lane);
      const v4u rec = {asu(o[0]), tag, asu(o[1]), tag};
      const int cg = c2 * (CB / 4) + (lane >> 1);
      __builtin_amdgcn_raw_buffer_store_b128(rec, rs, WS_H2 + cg * 1024 + (lane & 1) * 512 + h2 * 16, 0, AUX_SC1);
    }
  } else if (w == 1 && h2 < CB / 4) {
    const int cg = c2 * (CB / 4) + h2;
    v4u rr = __builtin_amdgcn_raw_buffer_load_b128(rs, WS_H2 + cg * 1024 + lane * 16, 0, AUX_SC1);
    for (unsigned spins = 0;; ++spins) {
      const bool ok = (rr[1] == tag) & (rr[3] == tag);
      if (__all(ok) || spins > SPIN_CAP) break;
      __builtin_amdgcn_s_sleep(POLL_SLEEP);
      if (!ok) rr = __builtin_amdgcn_raw_buffer_load_b128(rs, WS_H2 + cg * 1024 + lane * 16, 0, AUX_SC1);
    }
    float a0 = asf(rr[0]), a1 = asf(rr[2]);
    a0 += dpp<XOR1>(a0); a1 += dpp<XOR1>(a1);
    a0 += dpp<XOR2>(a0); a1 += dpp<XOR2>(a1);
    a0 += dpp<XOR7>(a0); a1 += dpp<XOR7>(a1);
    a0 += dpp<XOR8>(a0); a1 += dpp<XOR8>(a1);
    a0 = swap16_add(a0, a0);
    a1 = swap16_add(a1, a1);
    if ((lane & 31) == 0) {
      *reinterpret_cast<GAS unsigned*>((GAS unsigned short*)out_ + 4 * cg + 2 * (lane >> 5)) = cvtpk(a0, a1);
    }
    if (cg == 0 && lane == 0)
      __hip_atomic_store((GAS unsigned*)(ws_ + WS_EPOCH), tag, __ATOMIC_RELAXED, __HIP_MEMORY_SCOPE_AGENT);
  }
}

}

extern "C" void kernel_launch(void* const* d_in, const int* in_sizes, int n_in, void* d_out, int out_size, void* d_ws,
                              size_t ws_size, hipStream_t stream) {
  (void)in_sizes; (void)n_in; (void)out_size; (void)ws_size;
  decode_kernel<<<dim3(NH * NSPLIT), dim3(NTHR), 0, stream>>>(
      (const unsigned short*)d_in[0], (const unsigned short*)d_in[1], (const unsigned short*)d_in[2],
      (const unsigned short*)d_in[3], (const unsigned short*)d_in[4], (unsigned short*)d_out, (unsigned char*)d_ws);
}
```

```cpp
#include <hip/hip_runtime.h>
#include <hip/hip_bf16.h>
#include <stdint.h>

#define GAS __attribute__((address_space(1)))
#define LAS __attribute__((address_space(3)))
typedef unsigned v4u __attribute__((ext_vector_type(4)));
typedef unsigned v2u __attribute__((ext_vector_type(2)));
typedef float f32x2 __attribute__((ext_vector_type(2)));
typedef float f32x4 __attribute__((ext_vector_type(4)));
typedef __bf16 bf16x2 __attribute__((ext_vector_type(2)));

namespace {

constexpr int DM = 640, NH = 32, DH = 128, SK = 16384, WIN = 4096;
constexpr int NSPLIT = 8;
constexpr int NKV = 15;
constexpr int NTHR = (NKV + 1) * 64;
constexpr int UT = 32;
constexpr int NU = WIN / UT / NSPLIT;
constexpr int QROWS_WG = DM / NSPLIT;
constexpr int QROWS_LANE = QROWS_WG / 4;
constexpr int CB = DM / NSPLIT;
constexpr int WS_EPOCH = 0;
constexpr int WS_O = 32768;
constexpr int WS_Q = WS_O + NH * NSPLIT * 1024;
constexpr int WS_H2 = WS_Q + NH * NSPLIT * 1024;
constexpr int WS_END = WS_H2 + 40 * 4096;
constexpr int OOB = 0x7ffffff0;
constexpr unsigned SPIN_CAP = 1u << 18;
constexpr int AUX_SC1 = 16;

#define TH_X 12
#define AHEAD 2
#define WO_TURN 15
#define ROT_UNIT h
#define ROT_SLOT (h >> 2)
#define DRY 0
#define PIPE_POLL 0
#define POLL_GAP 4
#define STR2(x) #x
#define STR(x) STR2(x)

__device__ __forceinline__ float asf(unsigned u) { return __builtin_bit_cast(float, u); }
__device__ __forceinline__ unsigned asu(float f) { return __builtin_bit_cast(unsigned, f); }
__device__ __forceinline__ float dot2(unsigned a, unsigned b, float acc) {
  return __builtin_amdgcn_fdot2_f32_bf16(__builtin_bit_cast(bf16x2, a), __builtin_bit_cast(bf16x2, b), acc, false);
}
template <int CTRL> __device__ __forceinline__ float dpp(float x) {
  return asf((unsigned)__builtin_amdgcn_mov_dpp((int)asu(x), CTRL, 0xf, 0xf, true));
}
template <int CTRL> __device__ __forceinline__ unsigned dppu(unsigned x) {
  return (unsigned)__builtin_amdgcn_mov_dpp((int)x, CTRL, 0xf, 0xf, true);
}
constexpr int XOR1 = 0xB1, XOR2 = 0x4E, XOR7 = 0x141, XOR8 = 0x128;
__device__ __forceinline__ unsigned cvtpk(float lo, float hi) {
  f32x2 v = {lo, hi};
  return __builtin_bit_cast(unsigned, __builtin_convertvector(v, bf16x2));
}
__device__ __forceinline__ float bf16r(float x) { return asf(cvtpk(x, x) & 0xffff0000u); }
__device__ __forceinline__ float swap32_add(float a, float b) {
  auto r = __builtin_amdgcn_permlane32_swap(asu(a), asu(b), false, false);
  return asf(r[0]) + asf(r[1]);
}
__device__ __forceinline__ float swap16_add(float a, float b) {
  auto r = __builtin_amdgcn_permlane16_swap(asu(a), asu(b), false, false);
  return asf(r[0]) + asf(r[1]);
}
__device__ __forceinline__ float swap32_max(float a) {
  auto r = __builtin_amdgcn_permlane32_swap(asu(a), asu(a), false, false);
  return fmaxf(asf(r[0]), asf(r[1]));
}
__device__ __forceinline__ float swap16_max(float a) {
  auto r = __builtin_amdgcn_permlane16_swap(asu(a), asu(a), false, false);
  return fmaxf(asf(r[0]), asf(r[1]));
}
__device__ __forceinline__ v4u ld_stream(const GAS v4u* p) { return __builtin_nontemporal_load(p); }
#define SB() __builtin_amdgcn_sched_barrier(0)
#define STAMP(n)
#define STAMP_OUT()
#define LDS_ST(p, v) __hip_atomic_store((p), (v), __ATOMIC_RELAXED, __HIP_MEMORY_SCOPE_WORKGROUP)
#define LDS_LD(p) __hip_atomic_load((p), __ATOMIC_RELAXED, __HIP_MEMORY_SCOPE_WORKGROUP)
#define LDS_ADD(p, v) __hip_atomic_fetch_add((p), (v), __ATOMIC_RELAXED, __HIP_MEMORY_SCOPE_WORKGROUP)
__device__ __forceinline__ void wg_barrier() {
  SB();
  asm volatile("s_waitcnt lgkmcnt(0)" ::: "memory");
  __builtin_amdgcn_s_barrier();
  asm volatile("" ::: "memory");
  SB();
}
__device__ __forceinline__ void lds_wait_ge(LAS unsigned* p, int want) {
  for (unsigned spins = 0; spins < (SPIN_CAP << 4); ++spins) {
    if ((int)__builtin_amdgcn_readfirstlane(LDS_LD(p)) >= want) break;
    __builtin_amdgcn_s_sleep(1);
  }
  asm volatile("" ::: "memory");
}
__device__ __forceinline__ v4u ld_sc1(__amdgpu_buffer_rsrc_t rs, int off) {
  return __builtin_amdgcn_raw_buffer_load_b128(rs, off, 0, AUX_SC1);
}
#define POLL_LD(dst, off) asm volatile("buffer_load_dwordx4 %0, %1, %2, 0 offen sc1" : "+v"(dst) : "v"(off), "s"(srd) : "memory")
#define POLL_WAIT1(n, a) asm volatile("s_waitcnt vmcnt(" #n ")" : "+v"(a)::"memory")
#define POLL_WAIT2(n, a, b) asm volatile("s_waitcnt vmcnt(" #n ")" : "+v"(a), "+v"(b)::"memory")
#define POLL_DRAIN(a) asm volatile("s_waitcnt vmcnt(0)" : "+v"(a[0]), "+v"(a[1]), "+v"(a[2]), "+v"(a[3])::"memory")
__device__ __forceinline__ unsigned mix(unsigned b, unsigned x) { return b * 0x9E3779B1u ^ __builtin_amdgcn_alignbit(x, x, 13); }
__device__ __forceinline__ bool tagged(const v4u& r, unsigned tag) { return (r[1] == tag) & (r[3] == tag); }

__global__ void __launch_bounds__(NTHR, 4)
decode_kernel(const unsigned short* __restrict__ x_, const unsigned short* __restrict__ k_,
              const unsigned short* __restrict__ v_, const unsigned short* __restrict__ wq_,
              const unsigned short* __restrict__ wo_, unsigned short* __restrict__ out_, unsigned char* ws_) {
  __shared__ __attribute__((aligned(16))) float lds[16 * DH + 9 * DH + 64 + 64];
  float* redo = lds;
  float* redw = lds + 16 * DH;
  unsigned* qs = reinterpret_cast<unsigned*>(lds + 25 * DH);
  float* wml = lds + 25 * DH + 64;
  LAS unsigned* lflag = (LAS unsigned*)(wml + 32);
  STAMP(0);
  const int tid = threadIdx.x, lane = tid & 63;
  const int wv = __builtin_amdgcn_readfirstlane(tid >> 6);
  const int w = wv - 1;
  const int r = lane >> 4, c = lane & 15;
  const int b = blockIdx.x;
  const int h = b >> 3, s = b & 7;
  const int h2 = (b & 7) * 4 + ((b >> 3) & 3), c2 = b >> 5;
  constexpr float LOG2E = 1.4426950408889634f;

  const __amdgpu_buffer_rsrc_t rs = __builtin_amdgcn_make_buffer_rsrc((void*)ws_, 0, WS_END, 0x00020000);
  const v4u srd = {(unsigned)(uintptr_t)ws_, (unsigned)((uintptr_t)ws_ >> 32) & 0xffffu, (unsigned)WS_END, 0x00020000u};

  if (wv == 0) {
    __builtin_amdgcn_s_setprio(3);
    const unsigned ep = __hip_atomic_load((GAS unsigned*)(ws_ + WS_EPOCH), __ATOMIC_RELAXED, __HIP_MEMORY_SCOPE_AGENT);
    const GAS v2u* xp = (const GAS v2u*)(x_ + QROWS_WG * s + QROWS_LANE * r);
    v2u xv[5];
#pragma unroll
    for (int i = 0; i < 5; ++i) xv[i] = xp[i];
    SB();
    const GAS v4u* wqp = (const GAS v4u*)(wq_ + (size_t)(QROWS_WG * s + QROWS_LANE * r) * (NH * DH) + h * DH + 8 * c);
    v4u wq[QROWS_LANE];
#pragma unroll
    for (int i = 0; i < QROWS_LANE; ++i) wq[i] = ld_stream(wqp + (size_t)i * (NH * DH / 8));
    SB();
    if (lane < 8) LDS_ST(lflag + lane, 0u);
    wg_barrier();
    float qa[8];
#pragma unroll
    for (int k = 0; k < 8; ++k) qa[k] = 0.f;
#pragma unroll
    for (int j = 0; j < QROWS_LANE / 2; ++j) {
      const unsigned xpair = xv[j >> 1][j & 1];
#pragma unroll
      for (int k = 0; k < 4; ++k) {
        const unsigned a = wq[2 * j][k], bb = wq[2 * j + 1][k];
        qa[2 * k] = dot2(__builtin_amdgcn_perm(bb, a, 0x05040100u), xpair, qa[2 * k]);
        qa[2 * k + 1] = dot2(__builtin_amdgcn_perm(bb, a, 0x07060302u), xpair, qa[2 * k + 1]);
      }
    }
    const unsigned tg = ep + 1u;
    float u[4], t[2];
#pragma unroll
    for (int k = 0; k < 4; ++k) u[k] = swap32_add(qa[k], qa[k + 4]);
#pragma unroll
    for (int k = 0; k < 2; ++k) t[k] = swap16_add(u[k], u[k + 2]);
    const v4u rec = {asu(t[0]), tg, asu(t[1]), tg};
    __builtin_amdgcn_raw_buffer_store_b128(rec, rs, WS_Q + (h * NSPLIT + s) * 1024 + (8 * c + 2 * r) * 8, 0, AUX_SC1);
    STAMP(1);
    v4u g[NSPLIT];
#pragma unroll
    for (int j = 0; j < NSPLIT; ++j) g[j] = ld_sc1(rs, WS_Q + (h * NSPLIT + j) * 1024 + lane * 16);
    for (unsigned spins = 0;; ++spins) {
      bool ok = true;
#pragma unroll
      for (int j = 0; j < NSPLIT; ++j) ok &= tagged(g[j], tg);
      if (__all(ok) || spins > SPIN_CAP) break;
      __builtin_amdgcn_s_sleep(1);
#pragma unroll
      for (int j = 0; j < NSPLIT; ++j)
        if (!tagged(g[j], tg)) g[j] = ld_sc1(rs, WS_Q + (h * NSPLIT + j) * 1024 + lane * 16);
    }
    float q0 = 0.f, q1 = 0.f;
#pragma unroll
    for (int j = 0; j < NSPLIT; ++j) { q0 += asf(g[j][0]); q1 += asf(g[j][2]); }
    qs[lane] = cvtpk(q0, q1);
    if (lane == 0) LDS_ST(lflag + 1, tg);
    asm volatile("s_waitcnt lgkmcnt(0)" ::: "memory");
    if (lane == 0) LDS_ST(lflag, 1u);
    STAMP(2);
    {
      float M = -INFINITY, L = 0.f, o0 = 0.f, o1 = 0.f;
#pragma unroll 1
      for (int ww = 1; ww < NKV; ++ww) {
        for (unsigned spins = 0; spins < (SPIN_CAP << 4); ++spins)
          if ((__builtin_amdgcn_readfirstlane(LDS_LD(lflag + 3)) >> ww) & 1u) break;
        asm volatile("" ::: "memory");
        const f32x2 ml = *reinterpret_cast<const f32x2*>(wml + 2 * ww);
        const f32x2 ov = *reinterpret_cast<const f32x2*>(redo + ww * DH + 2 * lane);
        const float Mn = fmaxf(M, ml[0]);
        const float a = __builtin_amdgcn_exp2f((M - Mn) * LOG2E), f = __builtin_amdgcn_exp2f((ml[0] - Mn) * LOG2E);
        o0 = fmaf(o0, a, f * ov[0]);
        o1 = fmaf(o1, a, f * ov[1]);
        L = fmaf(L, a, f * ml[1]);
        M = Mn;
        asm volatile("" : "+v"(o0), "+v"(o1), "+v"(L), "+v"(M));
      }
      *reinterpret_cast<f32x2*>(redo + 2 * lane) = f32x2{o0, o1};
      if (lane == 0) *reinterpret_cast<f32x2*>(wml) = f32x2{M, L};
      asm volatile("s_waitcnt lgkmcnt(0)" ::: "memory");
      if (lane == 0) LDS_ST(lflag + 5, 1u);
      STAMP(7);
    }
    STAMP_OUT();
    return;
  }

  const size_t kvbase = ((size_t)h * SK + (SK - WIN) + s * (NU * UT)) * DH * 2;
  const GAS unsigned char* kp0 = (const GAS unsigned char*)k_ + kvbase;
  const GAS unsigned char* vp0 = (const GAS unsigned char*)v_ + kvbase;
  const unsigned loff = lane * 16;
  const int rot = ROT_SLOT;
  float mrun = -INFINITY, lrun = 0.f, acc[8];
#pragma unroll
  for (int k = 0; k < 8; ++k) acc[k] = 0.f;
  v4u qp = {0u, 0u, 0u, 0u};
  wg_barrier();
  for (int j = w; j < NU; j += NKV) {
    const int unit = (j + ROT_UNIT) & (NU - 1);
    if (j >= AHEAD) lds_wait_ge(lflag + 2, j - AHEAD + 1);
    if (j == w) STAMP(1);
    const GAS unsigned char* kp = kp0 + (size_t)unit * (UT * DH * 2);
    const GAS unsigned char* vp = vp0 + (size_t)unit * (UT * DH * 2);
    v4u kk[8], vv[8];
    SB();
#pragma unroll
    for (int i = 0; i < 8; ++i) kk[i] = ld_stream((const GAS v4u*)(kp + (((i + rot) & 7) << 10) + loff));
#pragma unroll
    for (int i = 0; i < 8; ++i) vv[i] = ld_stream((const GAS v4u*)(vp + (((i + rot) & 7) << 10) + loff));
    SB();
    asm volatile("s_waitcnt vmcnt(" STR(TH_X) ")" ::: "memory");
    if (lane == 0) LDS_ADD(lflag + 2, 1u);
    SB();
    STAMP(3);
    if (j == w) {
      lds_wait_ge(lflag, 1);
      qp = *reinterpret_cast<const v4u*>(qs + 4 * c);
      STAMP(4);
    }
    float sc[8];
#pragma unroll
    for (int i = 0; i < 8; ++i) {
      float d = 0.f;
#pragma unroll
      for (int k = 0; k < 4; ++k) d = dot2(kk[i][k], qp[k], d);
      sc[i] = d;
    }
#define TR_STEP(HALF, CTRL, BIT)                                  \
  _Pragma("unroll") for (int i = 0; i < (HALF); ++i) {            \
    const float lo_ = sc[i], hi_ = sc[i + (HALF)];                \
    const bool up_ = (c & (BIT)) != 0;                            \
    const float keep_ = up_ ? hi_ : lo_, send_ = up_ ? lo_ : hi_; \
    sc[i] = keep_ + dpp<CTRL>(send_);                             \
  }
    TR_STEP(4, XOR8, 8) TR_STEP(2, XOR7, 4) TR_STEP(1, XOR2, 2)
#undef TR_STEP
    const float sraw = sc[0] + dpp<XOR1>(sc[0]);
    const float sv = bf16r(bf16r(sraw) * 0.08838834764831845f);
    float mx = sv;
    mx = fmaxf(mx, dpp<XOR2>(mx));
    mx = fmaxf(mx, dpp<XOR7>(mx));
    mx = fmaxf(mx, dpp<XOR8>(mx));
    mx = swap16_max(mx);
    mx = swap32_max(mx);
    const float mnew = fmaxf(mrun, mx);
    const float alpha = __builtin_amdgcn_exp2f((mrun - mnew) * LOG2E);
    const float p = __builtin_amdgcn_exp2f((sv - mnew) * LOG2E);
    mrun = mnew;
    {
      float l = p;
      l += dpp<XOR2>(l);
      l += dpp<XOR7>(l);
      l += dpp<XOR8>(l);
      l = swap16_add(l, l);
      l = swap32_add(l, l);
      lrun = fmaf(lrun, alpha, l);
      if (lane == 0 && w != 0) *reinterpret_cast<f32x2*>(wml + 2 * w) = f32x2{mrun, lrun};
    }
#pragma unroll
    for (int k = 0; k < 8; ++k) acc[k] *= alpha;
#define PV_STEP(I)                                                       \
  {                                                                      \
    const float pi_ = dpp<0x150 + 2 * (I)>(p);                           \
    _Pragma("unroll") for (int k = 0; k < 4; ++k) {                      \
      const unsigned u_ = vv[I][k];                                      \
      acc[2 * k] = fmaf(pi_, asf(u_ << 16), acc[2 * k]);                 \
      acc[2 * k + 1] = fmaf(pi_, asf(u_ & 0xffff0000u), acc[2 * k + 1]); \
    }                                                                    \
    asm volatile("" : "+v"(acc[0]), "+v"(acc[1]), "+v"(acc[2]), "+v"(acc[3]), "+v"(acc[4]), "+v"(acc[5]), "+v"(acc[6]), "+v"(acc[7])); \
  }
    PV_STEP(0) PV_STEP(1) PV_STEP(2) PV_STEP(3) PV_STEP(4) PV_STEP(5) PV_STEP(6) PV_STEP(7)
#undef PV_STEP
    STAMP(5);
  }
  const unsigned tag = LDS_LD(lflag + 1);
  {
    float u[4], t[2];
#pragma unroll
    for (int k = 0; k < 4; ++k) u[k] = swap32_add(acc[k], acc[k + 4]);
#pragma unroll
    for (int k = 0; k < 2; ++k) t[k] = swap16_add(u[k], u[k + 2]);
    if (w != 0) {
      *reinterpret_cast<f32x2*>(redo + w * DH + 8 * c + 2 * r) = f32x2{t[0], t[1]};
      asm volatile("s_waitcnt lgkmcnt(0)" ::: "memory");
      if (lane == 0) LDS_ADD(lflag + 3, 1u << w);
    }
    if (w == 0 || (DRY && w == 12)) {
      for (unsigned spins = 0; spins < (SPIN_CAP << 4); ++spins)
        if (__builtin_amdgcn_readfirstlane(LDS_LD(lflag + (w == 0 ? 5 : 0))) != 0u) break;
      asm volatile("" ::: "memory");
      const f32x2 ml = *reinterpret_cast<const f32x2*>(wml);
      const f32x2 ov = *reinterpret_cast<const f32x2*>(redo + 8 * c + 2 * r);
      const float Mn = fmaxf(mrun, ml[0]);
      const float a = __builtin_amdgcn_exp2f((mrun - Mn) * LOG2E), f = __builtin_amdgcn_exp2f((ml[0] - Mn) * LOG2E);
      const unsigned o0 = asu(fmaf(t[0], a, f * ov[0])), o1 = asu(fmaf(t[1], a, f * ov[1]));
      const unsigned xw = asu((r & 1) ? fmaf(lrun, a, f * ml[1]) : Mn);
      const v4u rec1 = {o0, tag ^ mix(o1, xw), o1, xw};
      __builtin_amdgcn_raw_buffer_store_b128(rec1, rs, w == 0 ? WS_O + (h * NSPLIT + s) * 1024 + (8 * c + 2 * r) * 8 : OOB, 0, AUX_SC1);
    }
  }
  STAMP(6);
  if ((w >= 1 && w <= 8) || (DRY && w == 10)) {
    const bool dry = w == 10;
    const int tw = dry ? 8 : w - 1;
    const int ch = dry ? 0 : c < 10 ? c : 9;
    const GAS v4u* wop = (const GAS v4u*)(wo_ + (size_t)(h2 * DH + (dry ? 0 : 16 * tw + 4 * r)) * DM + c2 * CB + 8 * ch);
    lds_wait_ge(lflag + 2, dry ? 0 : WO_TURN);
    v4u wo[4];
#pragma unroll
    for (int jj = 0; jj < 4; ++jj) wo[jj] = ld_stream(wop + jj * (DM / 8));
    SB();
    const int sp = c & 7, pr = c >> 3;
    const int ooff = WS_O + (h2 * NSPLIT + sp) * 1024 + (16 * tw + 4 * r + 2 * pr) * 8;
    unsigned p01[8], p23[8];
#pragma unroll
    for (int k = 0; k < 4; ++k) {
      p01[2 * k] = __builtin_amdgcn_perm(wo[1][k], wo[0][k], 0x05040100u);
      p01[2 * k + 1] = __builtin_amdgcn_perm(wo[1][k], wo[0][k], 0x07060302u);
      p23[2 * k] = __builtin_amdgcn_perm(wo[3][k], wo[2][k], 0x05040100u);
      p23[2 * k + 1] = __builtin_amdgcn_perm(wo[3][k], wo[2][k], 0x07060302u);
    }
    asm volatile("" : "+v"(p01[0]), "+v"(p01[1]), "+v"(p01[2]), "+v"(p01[3]), "+v"(p01[4]), "+v"(p01[5]), "+v"(p01[6]), "+v"(p01[7]));
    asm volatile("" : "+v"(p23[0]), "+v"(p23[1]), "+v"(p23[2]), "+v"(p23[3]), "+v"(p23[4]), "+v"(p23[5]), "+v"(p23[6]), "+v"(p23[7]));
    unsigned bo0 = 0u, bo1 = 0u, bx = 0u;
    bool ho = dry;
    for (unsigned spins = 0; spins <= SPIN_CAP; ++spins) {
      const v4u ra = ld_sc1(rs, ho ? OOB : ooff);
      const bool to = ((ra[1] ^ mix(ra[2], ra[3])) == tag) & !ho;
      bo0 = to ? ra[0] : bo0; bo1 = to ? ra[2] : bo1; bx = to ? ra[3] : bx; ho |= to;
      if (__all(ho)) break;
      __builtin_amdgcn_s_sleep(1);
    }
    if (!dry) STAMP(8);
    const unsigned bxo = dppu<XOR8>(bx);
    const float Ms = asf(pr ? bxo : bx);
    const unsigned bl = pr ? bx : bxo;
    float Mh = Ms;
    Mh = fmaxf(Mh, dpp<XOR1>(Mh));
    Mh = fmaxf(Mh, dpp<XOR2>(Mh));
    Mh = fmaxf(Mh, dpp<XOR7>(Mh));
    const float ws = __builtin_amdgcn_exp2f((Ms - Mh) * LOG2E);
    float Lh = ws * asf(bl);
    float o0 = ws * asf(bo0), o1 = ws * asf(bo1);
    Lh += dpp<XOR1>(Lh); o0 += dpp<XOR1>(o0); o1 += dpp<XOR1>(o1);
    Lh += dpp<XOR2>(Lh); o0 += dpp<XOR2>(o0); o1 += dpp<XOR2>(o1);
    Lh += dpp<XOR7>(Lh); o0 += dpp<XOR7>(o0); o1 += dpp<XOR7>(o1);
    const float inv = __builtin_amdgcn_rcpf(Lh);
    const unsigned mine = cvtpk(o0 * inv, o1 * inv);
    const unsigned other = dppu<XOR8>(mine);
    const unsigned o01 = pr ? other : mine, o23 = pr ? mine : other;
    float g8[8];
#pragma unroll
    for (int k = 0; k < 8; ++k) g8[k] = dot2(p23[k], o23, dot2(p01[k], o01, 0.f));
    float u[4], t[2];
#pragma unroll
    for (int k = 0; k < 4; ++k) u[k] = swap32_add(g8[k], g8[k + 4]);
#pragma unroll
    for (int k = 0; k < 2; ++k) t[k] = swap16_add(u[k], u[k + 2]);
    *reinterpret_cast<f32x2*>(redw + tw * DH + 8 * c + 2 * r) = f32x2{t[0], t[1]};
    asm volatile("s_waitcnt lgkmcnt(0)" ::: "memory");
    unsigned old = 0u;
    if (lane == 0) old = LDS_ADD(lflag + (dry ? 6 : 4), 1u);
    old = __builtin_amdgcn_readfirstlane(old);
    asm volatile("" ::: "memory");
    if (old == (dry ? 0u : 7u) && lane < CB / 2) {
      f32x2 o = {0.f, 0.f};
#pragma unroll
      for (int ww = 0; ww < 8; ++ww) o += *reinterpret_cast<const f32x2*>(redw + ww * DH + 2 * lane);
      const v4u rec3 = {asu(o[0]), tag, asu(o[1]), tag};
      const int cg = c2 * (CB / 16) + (lane >> 3);
      __builtin_amdgcn_raw_buffer_store_b128(rec3, rs, dry ? OOB : WS_H2 + cg * 4096 + h2 * 128 + (lane & 7) * 16, 0, AUX_SC1);
    }
    if (!dry) STAMP(9);
  } else if ((w == 9 || (DRY && w == 11)) && h2 < CB / 16) {
    const bool dry = w == 11;
    const int cg = c2 * (CB / 16) + h2;
    const int goff = WS_H2 + cg * 4096 + lane * 16;
    lds_wait_ge(lflag + 2, dry ? 0 : NU);
    unsigned b0[4] = {0u, 0u, 0u, 0u}, b1[4] = {0u, 0u, 0u, 0u};
    bool hv[4] = {dry, dry, dry, dry};
    for (unsigned spins = 0; spins <= SPIN_CAP; ++spins) {
      v4u ra[4];
#pragma unroll
      for (int k = 0; k < 4; ++k) ra[k] = ld_sc1(rs, hv[k] ? OOB : goff + k * 1024);
#pragma unroll
      for (int k = 0; k < 4; ++k) {
        const bool tv = tagged(ra[k], tag) & !hv[k];
        b0[k] = tv ? ra[k][0] : b0[k]; b1[k] = tv ? ra[k][2] : b1[k]; hv[k] |= tv;
      }
      if (__all(hv[0] & hv[1] & hv[2] & hv[3])) break;
      __builtin_amdgcn_s_sleep(1);
    }
    float a0 = (asf(b0[0]) + asf(b0[1])) + (asf(b0[2]) + asf(b0[3]));
    float a1 = (asf(b1[0]) + asf(b1[1])) + (asf(b1[2]) + asf(b1[3]));
    a0 += dpp<XOR8>(a0); a1 += dpp<XOR8>(a1);
    a0 = swap16_add(a0, a0); a1 = swap16_add(a1, a1);
    a0 = swap32_add(a0, a0); a1 = swap32_add(a1, a1);
    const __amdgpu_buffer_rsrc_t ro = __builtin_amdgcn_make_buffer_rsrc((void*)out_, 0, DM * 2, 0x00020000);
    __builtin_amdgcn_raw_buffer_store_b32(cvtpk(a0, a1), ro, (lane < 8 && !dry) ? 32 * cg + 4 * lane : OOB, 0, 0);
    if (dry) {
      const unsigned junk = __builtin_amdgcn_raw_buffer_load_b32(ro, lane == 0 ? 32 * cg : OOB, 0, 0);
      asm volatile("" ::"v"(junk));
    }
    if (cg == 0 && lane == 0 && !dry)
      __hip_atomic_store((GAS unsigned*)(ws_ + WS_EPOCH), tag, __ATOMIC_RELAXED, __HIP_MEMORY_SCOPE_AGENT);
    if (!dry) STAMP(10);
  }
  STAMP_OUT();
}

}

extern "C" void kernel_launch(void* const* d_in, const int* in_sizes, int n_in, void* d_out, int out_size, void* d_ws,
                              size_t ws_size, hipStream_t stream) {
  (void)in_sizes; (void)n_in; (void)out_size; (void)ws_size;
  decode_kernel<<<dim3(NH * NSPLIT), dim3(NTHR), 0, stream>>>(
      (const unsigned short*)d_in[0], (const unsigned short*)d_in[1], (const unsigned short*)d_in[2],
      (const unsigned short*)d_in[3], (const unsigned short*)d_in[4], (unsigned short*)d_out, (unsigned char*)d_ws);
}
```

```cpp
#include <hip/hip_runtime.h>
#include <hip/hip_bf16.h>
#include <stdint.h>

#define GAS __attribute__((address_space(1)))
#define LAS __attribute__((address_space(3)))
typedef unsigned v4u __attribute__((ext_vector_type(4)));
typedef unsigned v2u __attribute__((ext_vector_type(2)));
typedef float f32x2 __attribute__((ext_vector_type(2)));
typedef float f32x4 __attribute__((ext_vector_type(4)));
typedef __bf16 bf16x2 __attribute__((ext_vector_type(2)));

namespace {

constexpr int DM = 640, NH = 32, DH = 128, SK = 16384, WIN = 4096;
constexpr int NSPLIT = 8;
constexpr int NKV = 15;
constexpr int NTHR = (NKV + 1) * 64;
constexpr int UT = 32;
constexpr int NU = WIN / UT / NSPLIT;
constexpr int QROWS_WG = DM / NSPLIT;
constexpr int QROWS_LANE = QROWS_WG / 4;
constexpr int CB = DM / NSPLIT;
constexpr int NREC = 1;
constexpr int WS_EPOCH = 0;
constexpr int WS_O = 32768;
constexpr int WS_Q = WS_O + NH * NSPLIT * NREC * 1024;
constexpr int WS_H2 = WS_Q + NH * NSPLIT * 1024;
constexpr int WS_END = WS_H2 + 40 * 4096;
constexpr int OOB = 0x7ffffff0;
constexpr unsigned SPIN_CAP = 1u << 18;
constexpr int AUX_SC1 = 16;

#define TH_X 12
#define AHEAD 3
#define WO_TURN 16
#define CODE_PF 11
#define BAL_MASK 1
#define GATE1 1
#define DRY 1
#define LDS_WAIT 0
#define STR2(x) #x
#define STR(x) STR2(x)

__device__ __forceinline__ float asf(unsigned u) { return __builtin_bit_cast(float, u); }
__device__ __forceinline__ unsigned asu(float f) { return __builtin_bit_cast(unsigned, f); }
__device__ __forceinline__ float dot2(unsigned a, unsigned b, float acc) {
  return __builtin_amdgcn_fdot2_f32_bf16(__builtin_bit_cast(bf16x2, a), __builtin_bit_cast(bf16x2, b), acc, false);
}
template <int CTRL> __device__ __forceinline__ float dpp(float x) {
  return asf((unsigned)__builtin_amdgcn_mov_dpp((int)asu(x), CTRL, 0xf, 0xf, true));
}
template <int CTRL> __device__ __forceinline__ unsigned dppu(unsigned x) {
  return (unsigned)__builtin_amdgcn_mov_dpp((int)x, CTRL, 0xf, 0xf, true);
}
constexpr int XOR1 = 0xB1, XOR2 = 0x4E, XOR7 = 0x141, XOR8 = 0x128;
__device__ __forceinline__ unsigned cvtpk(float lo, float hi) {
  f32x2 v = {lo, hi};
  return __builtin_bit_cast(unsigned, __builtin_convertvector(v, bf16x2));
}
__device__ __forceinline__ float bf16r(float x) { return asf(cvtpk(x, x) & 0xffff0000u); }
__device__ __forceinline__ float swap32_add(float a, float b) {
  auto r = __builtin_amdgcn_permlane32_swap(asu(a), asu(b), false, false);
  return asf(r[0]) + asf(r[1]);
}
__device__ __forceinline__ float swap16_add(float a, float b) {
  auto r = __builtin_amdgcn_permlane16_swap(asu(a), asu(b), false, false);
  return asf(r[0]) + asf(r[1]);
}
__device__ __forceinline__ float swap32_max(float a) {
  auto r = __builtin_amdgcn_permlane32_swap(asu(a), asu(a), false, false);
  return fmaxf(asf(r[0]), asf(r[1]));
}
__device__ __forceinline__ float swap16_max(float a) {
  auto r = __builtin_amdgcn_permlane16_swap(asu(a), asu(a), false, false);
  return fmaxf(asf(r[0]), asf(r[1]));
}
__device__ __forceinline__ v4u ld_stream(const GAS v4u* p) { return __builtin_nontemporal_load(p); }
#define SB() __builtin_amdgcn_sched_barrier(0)
#define STAMP(n)
#define STAMP_OUT()
#define LDS_ST(p, v) __hip_atomic_store((p), (v), __ATOMIC_RELAXED, __HIP_MEMORY_SCOPE_WORKGROUP)
#define LDS_LD(p) __hip_atomic_load((p), __ATOMIC_RELAXED, __HIP_MEMORY_SCOPE_WORKGROUP)
#define LDS_ADD(p, v) __hip_atomic_fetch_add((p), (v), __ATOMIC_RELAXED, __HIP_MEMORY_SCOPE_WORKGROUP)
#define LDS_ORDER() asm volatile("" ::: "memory")
__device__ __forceinline__ void wg_barrier() {
  SB();
  asm volatile("s_waitcnt lgkmcnt(0)" ::: "memory");
  __builtin_amdgcn_s_barrier();
  asm volatile("" ::: "memory");
  SB();
}
__device__ __forceinline__ void lds_wait_ge(LAS unsigned* p, int want) {
  for (unsigned spins = 0; spins < (SPIN_CAP << 4); ++spins) {
    if ((int)__builtin_amdgcn_readfirstlane(LDS_LD(p)) >= want) break;
    __builtin_amdgcn_s_sleep(1);
  }
  asm volatile("" ::: "memory");
}
__device__ __forceinline__ v4u ld_sc1(__amdgpu_buffer_rsrc_t rs, int off) {
  return __builtin_amdgcn_raw_buffer_load_b128(rs, off, 0, AUX_SC1);
}
__device__ __forceinline__ unsigned mix(unsigned b, unsigned x) { return b * 0x9E3779B1u ^ __builtin_amdgcn_alignbit(x, x, 13); }
__device__ __forceinline__ bool tagged(const v4u& r, unsigned tag) { return (r[1] == tag) & (r[3] == tag); }

__global__ void __launch_bounds__(NTHR, 4)
decode_kernel(const unsigned short* __restrict__ x_, const unsigned short* __restrict__ k_,
              const unsigned short* __restrict__ v_, const unsigned short* __restrict__ wq_,
              const unsigned short* __restrict__ wo_, unsigned short* __restrict__ out_, unsigned char* ws_) {
  __shared__ __attribute__((aligned(16))) float lds[17 * DH + 9 * DH + 64 + 64 + 16];
  float* redo = lds;
  float* redw = lds + 17 * DH;
  unsigned* qs = reinterpret_cast<unsigned*>(lds + 26 * DH);
  float* wml = lds + 26 * DH + 64;
  LAS unsigned* lflag = (LAS unsigned*)(wml + 40);
  STAMP(0);
  {
    unsigned long long pc;
    asm volatile("s_getpc_b64 %0" : "=s"(pc));
    if (threadIdx.x >= NTHR - 64) {
      const GAS v4u* cp = (const GAS v4u*)((pc & ~255ull) + ((blockIdx.x >> 3) % CODE_PF) * 1024 + (threadIdx.x & 63) * 16);
      const v4u junk = *cp;
      asm volatile("" ::"v"(junk));
    }
  }
  asm volatile("" ::"s"(x_), "s"(k_), "s"(v_), "s"(wq_), "s"(wo_), "s"(out_), "s"(ws_));
  const int tid = threadIdx.x, lane = tid & 63;
  const int wv = __builtin_amdgcn_readfirstlane(tid >> 6);
  const int w = wv - 1;
  const int r = lane >> 4, c = lane & 15;
  const int b = blockIdx.x;
  const int h = b >> 3, s = b & 7;
  const int h2 = (b & 7) * 4 + ((b >> 3) & 3), c2 = b >> 5;
  constexpr float LOG2E = 1.4426950408889634f;
  const int nu = NU + ((h & BAL_MASK) == BAL_MASK && BAL_MASK >= 0 ? ((s & 1) ? -1 : 1) : 0);

  const __amdgpu_buffer_rsrc_t rs = __builtin_amdgcn_make_buffer_rsrc((void*)ws_, 0, WS_END, 0x00020000);

  if (__builtin_expect(wv == 0, 1)) {
    __builtin_amdgcn_s_setprio(3);
    const unsigned ep = __hip_atomic_load((GAS unsigned*)(ws_ + WS_EPOCH), __ATOMIC_RELAXED, __HIP_MEMORY_SCOPE_AGENT);
    const GAS v2u* xp = (const GAS v2u*)(x_ + QROWS_WG * s + QROWS_LANE * r);
    v2u xv[5];
#pragma unroll
    for (int i = 0; i < 5; ++i) xv[i] = xp[i];
    SB();
    const GAS v4u* wqp = (const GAS v4u*)(wq_ + (size_t)(QROWS_WG * s + QROWS_LANE * r) * (NH * DH) + h * DH + 8 * c);
    v4u wq[QROWS_LANE];
#pragma unroll
    for (int i = 0; i < QROWS_LANE; ++i) wq[i] = ld_stream(wqp + (size_t)i * (NH * DH / 8));
    SB();
    if (lane < 16) LDS_ST(lflag + lane, 0u);
    wg_barrier();
    float qa[8];
#pragma unroll
    for (int k = 0; k < 8; ++k) qa[k] = 0.f;
#pragma unroll
    for (int j = 0; j < QROWS_LANE / 2; ++j) {
      const unsigned xpair = xv[j >> 1][j & 1];
#pragma unroll
      for (int k = 0; k < 4; ++k) {
        const unsigned a = wq[2 * j][k], bb = wq[2 * j + 1][k];
        qa[2 * k] = dot2(__builtin_amdgcn_perm(bb, a, 0x05040100u), xpair, qa[2 * k]);
        qa[2 * k + 1] = dot2(__builtin_amdgcn_perm(bb, a, 0x07060302u), xpair, qa[2 * k + 1]);
      }
    }
    const unsigned tg = ep + 1u;
    float u[4], t[2];
#pragma unroll
    for (int k = 0; k < 4; ++k) u[k] = swap32_add(qa[k], qa[k + 4]);
#pragma unroll
    for (int k = 0; k < 2; ++k) t[k] = swap16_add(u[k], u[k + 2]);
    const v4u rec = {asu(t[0]), tg, asu(t[1]), tg};
    __builtin_amdgcn_raw_buffer_store_b128(rec, rs, WS_Q + (h * NSPLIT + s) * 1024 + (8 * c + 2 * r) * 8, 0, AUX_SC1);
    STAMP(1);
    v4u g[NSPLIT];
#pragma unroll
    for (int j = 0; j < NSPLIT; ++j) g[j] = ld_sc1(rs, WS_Q + (h * NSPLIT + j) * 1024 + lane * 16);
    for (unsigned spins = 0;; ++spins) {
      bool ok = true;
#pragma unroll
      for (int j = 0; j < NSPLIT; ++j) ok &= tagged(g[j], tg);
      if (__all(ok) || spins > SPIN_CAP) break;
      __builtin_amdgcn_s_sleep(1);
#pragma unroll
      for (int j = 0; j < NSPLIT; ++j)
        if (!tagged(g[j], tg)) g[j] = ld_sc1(rs, WS_Q + (h * NSPLIT + j) * 1024 + lane * 16);
    }
    float q0 = 0.f, q1 = 0.f;
#pragma unroll
    for (int j = 0; j < NSPLIT; ++j) { q0 += asf(g[j][0]); q1 += asf(g[j][2]); }
    qs[lane] = cvtpk(q0, q1);
    if (lane == 0) LDS_ST(lflag + 1, tg);
    LDS_ORDER();
    if (lane == 0) LDS_ST(lflag, 1u);
    STAMP(2);
    float M = -INFINITY, L = 0.f, o0 = 0.f, o1 = 0.f;
#pragma unroll 1
    for (int j = 0; j < nu; ++j) {
      for (unsigned spins = 0; spins < (SPIN_CAP << 4); ++spins)
        if ((__builtin_amdgcn_readfirstlane(LDS_LD(lflag + 3)) >> j) & 1u) break;
      asm volatile("" ::: "memory");
      const f32x2 ml = *reinterpret_cast<const f32x2*>(wml + 2 * j);
      const f32x2 ov = *reinterpret_cast<const f32x2*>(redo + j * DH + 2 * lane);
      const float Mn = fmaxf(M, ml[0]);
      const float a = __builtin_amdgcn_exp2f((M - Mn) * LOG2E), f = __builtin_amdgcn_exp2f((ml[0] - Mn) * LOG2E);
      o0 = fmaf(o0, a, f * ov[0]);
      o1 = fmaf(o1, a, f * ov[1]);
      L = fmaf(L, a, f * ml[1]);
      M = Mn;
      asm volatile("" : "+v"(o0), "+v"(o1), "+v"(L), "+v"(M));
    }
    const unsigned xw = asu((lane & 1) ? L : M);
    const v4u rec1 = {asu(o0), tg ^ mix(asu(o1), xw), asu(o1), xw};
    __builtin_amdgcn_raw_buffer_store_b128(rec1, rs, WS_O + (h * NSPLIT + s) * (NREC * 1024) + lane * 16, 0, AUX_SC1);
    if (lane == 0) LDS_ST(lflag + 5, 1u);
    STAMP(7);
    STAMP_OUT();
    return;
  }

  const size_t kvbase = ((size_t)h * SK + (SK - WIN)) * DH * 2;
  const GAS unsigned char* kp0 = (const GAS unsigned char*)k_ + kvbase;
  const GAS unsigned char* vp0 = (const GAS unsigned char*)v_ + kvbase;
  const unsigned loff = lane * 16;
  const int rot = h >> 2;
  v4u qp = {0u, 0u, 0u, 0u};
  unsigned tag = 0u;
  if (w >= AHEAD) wg_barrier();
  for (int j = w; j < nu; j += NKV) {
    const int jj = j < NU ? j : NU - 1, ss = j < NU ? s : s + 1;
    const int unit = ((jj + h) & (NU - 1)) * NSPLIT + ((ss + jj) & 7);
    if (j >= AHEAD) lds_wait_ge(lflag + 2, j - AHEAD + 1);
    if (j == w) STAMP(1);
    const GAS unsigned char* kp = kp0 + (size_t)unit * (UT * DH * 2);
    const GAS unsigned char* vp = vp0 + (size_t)unit * (UT * DH * 2);
    v4u kk[8], vv[8];
    SB();
#pragma unroll
    for (int i = 0; i < 8; ++i) kk[i] = ld_stream((const GAS v4u*)(kp + (((i + rot) & 7) << 10) + loff));
#pragma unroll
    for (int i = 0; i < 8; ++i) vv[i] = ld_stream((const GAS v4u*)(vp + (((i + rot) & 7) << 10) + loff));
    SB();
    if (j < AHEAD) wg_barrier();
    asm volatile("s_waitcnt vmcnt(" STR(TH_X) ")" ::: "memory");
    if (lane == 0) LDS_ADD(lflag + 2, 1u);
    SB();
    STAMP(3);
    if (j == w) {
      lds_wait_ge(lflag, 1);
      qp = *reinterpret_cast<const v4u*>(qs + 4 * c);
      tag = LDS_LD(lflag + 1);
      STAMP(4);
    }
    float sc[8];
#pragma unroll
    for (int i = 0; i < 8; ++i) {
      float d = 0.f;
#pragma unroll
      for (int k = 0; k < 4; ++k) d = dot2(kk[i][k], qp[k], d);
      sc[i] = d;
    }
#define TR_STEP(HALF, CTRL, BIT)                                  \
  _Pragma("unroll") for (int i = 0; i < (HALF); ++i) {            \
    const float lo_ = sc[i], hi_ = sc[i + (HALF)];                \
    const bool up_ = (c & (BIT)) != 0;                            \
    const float keep_ = up_ ? hi_ : lo_, send_ = up_ ? lo_ : hi_; \
    sc[i] = keep_ + dpp<CTRL>(send_);                             \
  }
    TR_STEP(4, XOR8, 8) TR_STEP(2, XOR7, 4) TR_STEP(1, XOR2, 2)
#undef TR_STEP
    const float sraw = sc[0] + dpp<XOR1>(sc[0]);
    const float sv = bf16r(bf16r(sraw) * 0.08838834764831845f);
    float mx = sv;
    mx = fmaxf(mx, dpp<XOR2>(mx));
    mx = fmaxf(mx, dpp<XOR7>(mx));
    mx = fmaxf(mx, dpp<XOR8>(mx));
    mx = swap16_max(mx);
    mx = swap32_max(mx);
    const float p = __builtin_amdgcn_exp2f((sv - mx) * LOG2E);
    float l = p;
    l += dpp<XOR2>(l);
    l += dpp<XOR7>(l);
    l += dpp<XOR8>(l);
    l = swap16_add(l, l);
    l = swap32_add(l, l);
    if (lane == 0) *reinterpret_cast<f32x2*>(wml + 2 * j) = f32x2{mx, l};
    float acc[8];
#pragma unroll
    for (int k = 0; k < 8; ++k) acc[k] = 0.f;
#define PV_STEP(I)                                                       \
  {                                                                      \
    const float pi_ = dpp<0x150 + 2 * (I)>(p);                           \
    _Pragma("unroll") for (int k = 0; k < 4; ++k) {                      \
      const unsigned u_ = vv[I][k];                                      \
      acc[2 * k] = fmaf(pi_, asf(u_ << 16), acc[2 * k]);                 \
      acc[2 * k + 1] = fmaf(pi_, asf(u_ & 0xffff0000u), acc[2 * k + 1]); \
    }                                                                    \
    asm volatile("" : "+v"(acc[0]), "+v"(acc[1]), "+v"(acc[2]), "+v"(acc[3]), "+v"(acc[4]), "+v"(acc[5]), "+v"(acc[6]), "+v"(acc[7])); \
  }
    PV_STEP(0) PV_STEP(1) PV_STEP(2) PV_STEP(3) PV_STEP(4) PV_STEP(5) PV_STEP(6) PV_STEP(7)
#undef PV_STEP
    STAMP(5);
    float u[4], t[2];
#pragma unroll
    for (int k = 0; k < 4; ++k) u[k] = swap32_add(acc[k], acc[k + 4]);
#pragma unroll
    for (int k = 0; k < 2; ++k) t[k] = swap16_add(u[k], u[k + 2]);
    *reinterpret_cast<f32x2*>(redo + j * DH + 8 * c + 2 * r) = f32x2{t[0], t[1]};
    LDS_ORDER();
    if (lane == 0) LDS_ADD(lflag + 3, 1u << j);
    STAMP(6);
  }
  if ((w >= 2 && w <= 9) || (DRY && w == 11)) {
    const bool dry = w == 11;
    const int tw = dry ? 8 : w - 2;
    const int ch = dry ? 0 : c < 10 ? c : 9;
    const GAS v4u* wop = (const GAS v4u*)(wo_ + (size_t)(h2 * DH + (dry ? 0 : 16 * tw + 4 * r)) * DM + c2 * CB + 8 * ch);
    lds_wait_ge(lflag + 2, dry ? 0 : nu);
    v4u wo[4];
#pragma unroll
    for (int jj = 0; jj < 4; ++jj) wo[jj] = ld_stream(wop + jj * (DM / 8));
    SB();
    const int sp = c & 7, pr = c >> 3;
    const int ooff = WS_O + (h2 * NSPLIT + sp) * (NREC * 1024) + (16 * tw + 4 * r + 2 * pr) * 8;
    unsigned p01[8], p23[8];
#pragma unroll
    for (int k = 0; k < 4; ++k) {
      p01[2 * k] = __builtin_amdgcn_perm(wo[1][k], wo[0][k], 0x05040100u);
      p01[2 * k + 1] = __builtin_amdgcn_perm(wo[1][k], wo[0][k], 0x07060302u);
      p23[2 * k] = __builtin_amdgcn_perm(wo[3][k], wo[2][k], 0x05040100u);
      p23[2 * k + 1] = __builtin_amdgcn_perm(wo[3][k], wo[2][k], 0x07060302u);
    }
    asm volatile("" : "+v"(p01[0]), "+v"(p01[1]), "+v"(p01[2]), "+v"(p01[3]), "+v"(p01[4]), "+v"(p01[5]), "+v"(p01[6]), "+v"(p01[7]));
    asm volatile("" : "+v"(p23[0]), "+v"(p23[1]), "+v"(p23[2]), "+v"(p23[3]), "+v"(p23[4]), "+v"(p23[5]), "+v"(p23[6]), "+v"(p23[7]));
    lds_wait_ge(lflag + 5, dry ? 0 : 1);
    unsigned bo0[NREC], bo1[NREC], bx[NREC];
    int go[NREC];
#pragma unroll
    for (int k = 0; k < NREC; ++k) { bo0[k] = 0u; bo1[k] = 0u; bx[k] = 0u; go[k] = dry ? OOB : ooff + k * 1024; }
    for (unsigned spins = 0; spins <= SPIN_CAP; ++spins) {
      v4u ra[NREC];
#pragma unroll
      for (int k = 0; k < NREC; ++k) { asm volatile("" : "+v"(go[k])); ra[k] = ld_sc1(rs, go[k]); }
      SB();
      bool all = true;
#pragma unroll
      for (int k = 0; k < NREC; ++k) {
        const bool to = ((ra[k][1] ^ mix(ra[k][2], ra[k][3])) == tag) & (go[k] != OOB);
        bo0[k] = to ? ra[k][0] : bo0[k]; bo1[k] = to ? ra[k][2] : bo1[k]; bx[k] = to ? ra[k][3] : bx[k];
        go[k] = to ? OOB : go[k];
        all &= go[k] == OOB;
      }
      if (__all(all)) break;
      __builtin_amdgcn_s_sleep(1);
    }
    if (!dry) STAMP(8);
    float ms[NREC], ls[NREC], Mh = -INFINITY;
#pragma unroll
    for (int k = 0; k < NREC; ++k) {
      const unsigned bxo = dppu<XOR8>(bx[k]);
      ms[k] = asf(pr ? bxo : bx[k]);
      ls[k] = asf(pr ? bx[k] : bxo);
      Mh = fmaxf(Mh, ms[k]);
    }
    Mh = fmaxf(Mh, dpp<XOR1>(Mh));
    Mh = fmaxf(Mh, dpp<XOR2>(Mh));
    Mh = fmaxf(Mh, dpp<XOR7>(Mh));
    float Lh = 0.f, o0 = 0.f, o1 = 0.f;
#pragma unroll
    for (int k = 0; k < NREC; ++k) {
      const float ws = __builtin_amdgcn_exp2f((ms[k] - Mh) * LOG2E);
      Lh = fmaf(ws, ls[k], Lh);
      o0 = fmaf(ws, asf(bo0[k]), o0);
      o1 = fmaf(ws, asf(bo1[k]), o1);
    }
    Lh += dpp<XOR1>(Lh); o0 += dpp<XOR1>(o0); o1 += dpp<XOR1>(o1);
    Lh += dpp<XOR2>(Lh); o0 += dpp<XOR2>(o0); o1 += dpp<XOR2>(o1);
    Lh += dpp<XOR7>(Lh); o0 += dpp<XOR7>(o0); o1 += dpp<XOR7>(o1);
    const float inv = __builtin_amdgcn_rcpf(Lh);
    const unsigned mine = cvtpk(o0 * inv, o1 * inv);
    const unsigned other = dppu<XOR8>(mine);
    const unsigned o01 = pr ? other : mine, o23 = pr ? mine : other;
    float g8[8];
#pragma unroll
    for (int k = 0; k < 8; ++k) g8[k] = dot2(p23[k], o23, dot2(p01[k], o01, 0.f));
    float u[4], t[2];
#pragma unroll
    for (int k = 0; k < 4; ++k) u[k] = swap32_add(g8[k], g8[k + 4]);
#pragma unroll
    for (int k = 0; k < 2; ++k) t[k] = swap16_add(u[k], u[k + 2]);
    *reinterpret_cast<f32x2*>(redw + tw * DH + 8 * c + 2 * r) = f32x2{t[0], t[1]};
    LDS_ORDER();
    unsigned old = 0u;
    if (lane == 0) old = LDS_ADD(lflag + (dry ? 6 : 4), 1u);
    old = __builtin_amdgcn_readfirstlane(old);
    asm volatile("" ::: "memory");
    if (old == (dry ? 0u : 7u) && lane < CB / 2) {
      f32x2 o = {0.f, 0.f};
#pragma unroll
      for (int ww = 0; ww < 8; ++ww) o += *reinterpret_cast<const f32x2*>(redw + ww * DH + 2 * lane);
      const v4u rec3 = {asu(o[0]), tag, asu(o[1]), tag};
      const int cg = c2 * (CB / 16) + (lane >> 3);
      __builtin_amdgcn_raw_buffer_store_b128(rec3, rs, dry ? OOB : WS_H2 + cg * 4096 + h2 * 128 + (lane & 7) * 16, 0, AUX_SC1);
    }
    if (!dry) STAMP(9);
  } else if ((w == 10 || (DRY && w == 12)) && h2 < CB / 16) {
    const bool dry = w == 12;
    const int cg = c2 * (CB / 16) + h2;
    const int goff = WS_H2 + cg * 4096 + lane * 16;
    lds_wait_ge(lflag + 4, dry ? 0 : 1);
    unsigned b0[4] = {0u, 0u, 0u, 0u}, b1[4] = {0u, 0u, 0u, 0u};
    int go[4];
#pragma unroll
    for (int k = 0; k < 4; ++k) go[k] = dry ? OOB : goff + k * 1024;
    for (unsigned spins = 0; spins <= SPIN_CAP; ++spins) {
      asm volatile("" : "+v"(go[0]), "+v"(go[1]), "+v"(go[2]), "+v"(go[3]));
      v4u ra[4];
#pragma unroll
      for (int k = 0; k < 4; ++k) ra[k] = ld_sc1(rs, go[k]);
      SB();
      bool all = true;
#pragma unroll
      for (int k = 0; k < 4; ++k) {
        const bool tv = tagged(ra[k], tag) & (go[k] != OOB);
        b0[k] = tv ? ra[k][0] : b0[k]; b1[k] = tv ? ra[k][2] : b1[k];
        go[k] = tv ? OOB : go[k];
        all &= go[k] == OOB;
      }
      if (__all(all)) break;
      __builtin_amdgcn_s_sleep(1);
    }
    float a0 = (asf(b0[0]) + asf(b0[1])) + (asf(b0[2]) + asf(b0[3]));
    float a1 = (asf(b1[0]) + asf(b1[1])) + (asf(b1[2]) + asf(b1[3]));
    a0 += dpp<XOR8>(a0); a1 += dpp<XOR8>(a1);
    a0 = swap16_add(a0, a0); a1 = swap16_add(a1, a1);
    a0 = swap32_add(a0, a0); a1 = swap32_add(a1, a1);
    const __amdgpu_buffer_rsrc_t ro = __builtin_amdgcn_make_buffer_rsrc((void*)out_, 0, DM * 2, 0x00020000);
    __builtin_amdgcn_raw_buffer_store_b32(cvtpk(a0, a1), ro, (lane < 8 && !dry) ? 32 * cg + 4 * lane : OOB, 0, 0);
    if (dry) {
      const unsigned junk = __builtin_amdgcn_raw_buffer_load_b32(ro, lane == 0 ? 32 * cg : OOB, 0, 0);
      asm volatile("" ::"v"(junk));
    }
    if (cg == 0 && lane == 0 && !dry)
      __hip_atomic_store((GAS unsigned*)(ws_ + WS_EPOCH), tag, __ATOMIC_RELAXED, __HIP_MEMORY_SCOPE_AGENT);
    if (!dry) STAMP(10);
  }
  STAMP_OUT();
}

}

extern "C" void kernel_launch(void* const* d_in, const int* in_sizes, int n_in, void* d_out, int out_size, void* d_ws,
                              size_t ws_size, hipStream_t stream) {
  (void)in_sizes; (void)n_in; (void)out_size; (void)ws_size;
  decode_kernel<<<dim3(NH * NSPLIT), dim3(NTHR), 0, stream>>>(
      (const unsigned short*)d_in[0], (const unsigned short*)d_in[1], (const unsigned short*)d_in[2],
      (const unsigned short*)d_in[3], (const unsigned short*)d_in[4], (unsigned short*)d_out, (unsigned char*)d_ws);
}
```

```cpp
#include <hip/hip_runtime.h>
#include <hip/hip_bf16.h>
#include <stdint.h>

#define GAS __attribute__((address_space(1)))
#define LAS __attribute__((address_space(3)))
typedef unsigned v4u __attribute__((ext_vector_type(4)));
typedef unsigned v2u __attribute__((ext_vector_type(2)));
typedef float f32x2 __attribute__((ext_vector_type(2)));
typedef float f32x4 __attribute__((ext_vector_type(4)));
typedef __bf16 bf16x2 __attribute__((ext_vector_type(2)));

namespace {

constexpr int DM = 640, NH = 32, DH = 128, SK = 16384, WIN = 4096;
constexpr int NSPLIT = 8;
constexpr int NKV = 15;
constexpr int NTHR = (NKV + 1) * 64;
constexpr int UT = 32;
constexpr int NU = WIN / UT / NSPLIT;
constexpr int QROWS_WG = DM / NSPLIT;
constexpr int QROWS_LANE = QROWS_WG / 4;
constexpr int CB = DM / NSPLIT;
constexpr int NREC = 1;
constexpr int WS_EPOCH = 0;
constexpr int WS_O = 32768;
constexpr int WS_Q = WS_O + NH * NSPLIT * NREC * 1024;
constexpr int WS_H2 = WS_Q + NH * NSPLIT * 1024;
constexpr int WS_END = WS_H2 + 40 * 4096;
constexpr int OOB = 0x7ffffff0;
constexpr unsigned SPIN_CAP = 1u << 18;
constexpr int AUX_SC1 = 16;

#define TH_X 12
#define AHEAD 3
#define WO_TURN 16
#define CODE_PF 0
#define BAL_MASK 1
#define GATE1 1
#define DRY 1
#define LDS_WAIT 0
#define STR2(x) #x
#define STR(x) STR2(x)

__device__ __forceinline__ float asf(unsigned u) { return __builtin_bit_cast(float, u); }
__device__ __forceinline__ unsigned asu(float f) { return __builtin_bit_cast(unsigned, f); }
__device__ __forceinline__ float dot2(unsigned a, unsigned b, float acc) {
  return __builtin_amdgcn_fdot2_f32_bf16(__builtin_bit_cast(bf16x2, a), __builtin_bit_cast(bf16x2, b), acc, false);
}
template <int CTRL> __device__ __forceinline__ float dpp(float x) {
  return asf((unsigned)__builtin_amdgcn_mov_dpp((int)asu(x), CTRL, 0xf, 0xf, true));
}
template <int CTRL> __device__ __forceinline__ unsigned dppu(unsigned x) {
  return (unsigned)__builtin_amdgcn_mov_dpp((int)x, CTRL, 0xf, 0xf, true);
}
constexpr int XOR1 = 0xB1, XOR2 = 0x4E, XOR7 = 0x141, XOR8 = 0x128;
__device__ __forceinline__ unsigned cvtpk(float lo, float hi) {
  f32x2 v = {lo, hi};
  return __builtin_bit_cast(unsigned, __builtin_convertvector(v, bf16x2));
}
__device__ __forceinline__ float bf16r(float x) { return asf(cvtpk(x, x) & 0xffff0000u); }
__device__ __forceinline__ float swap32_add(float a, float b) {
  auto r = __builtin_amdgcn_permlane32_swap(asu(a), asu(b), false, false);
  return asf(r[0]) + asf(r[1]);
}
__device__ __forceinline__ float swap16_add(float a, float b) {
  auto r = __builtin_amdgcn_permlane16_swap(asu(a), asu(b), false, false);
  return asf(r[0]) + asf(r[1]);
}
__device__ __forceinline__ float swap32_max(float a) {
  auto r = __builtin_amdgcn_permlane32_swap(asu(a), asu(a), false, false);
  return fmaxf(asf(r[0]), asf(r[1]));
}
__device__ __forceinline__ float swap16_max(float a) {
  auto r = __builtin_amdgcn_permlane16_swap(asu(a), asu(a), false, false);
  return fmaxf(asf(r[0]), asf(r[1]));
}
__device__ __forceinline__ v4u ld_stream(const GAS v4u* p) { return __builtin_nontemporal_load(p); }
#define SB() __builtin_amdgcn_sched_barrier(0)
#define STAMP(n)
#define STAMP_OUT()
#define LDS_ST(p, v) __hip_atomic_store((p), (v), __ATOMIC_RELAXED, __HIP_MEMORY_SCOPE_WORKGROUP)
#define LDS_LD(p) __hip_atomic_load((p), __ATOMIC_RELAXED, __HIP_MEMORY_SCOPE_WORKGROUP)
#define LDS_ADD(p, v) __hip_atomic_fetch_add((p), (v), __ATOMIC_RELAXED, __HIP_MEMORY_SCOPE_WORKGROUP)
#define LDS_ORDER() asm volatile("" ::: "memory")
__device__ __forceinline__ void wg_barrier() {
  SB();
  asm volatile("s_waitcnt lgkmcnt(0)" ::: "memory");
  __builtin_amdgcn_s_barrier();
  asm volatile("" ::: "memory");
  SB();
}
__device__ __forceinline__ void lds_wait_ge(LAS unsigned* p, int want) {
  for (unsigned spins = 0; spins < (SPIN_CAP << 4); ++spins) {
    if ((int)__builtin_amdgcn_readfirstlane(LDS_LD(p)) >= want) break;
    __builtin_amdgcn_s_sleep(1);
  }
  asm volatile("" ::: "memory");
}
__device__ __forceinline__ v4u ld_sc1(__amdgpu_buffer_rsrc_t rs, int off) {
  return __builtin_amdgcn_raw_buffer_load_b128(rs, off, 0, AUX_SC1);
}
__device__ __forceinline__ unsigned mix(unsigned b, unsigned x) { return b * 0x9E3779B1u ^ __builtin_amdgcn_alignbit(x, x, 13); }
__device__ __forceinline__ bool tagged(const v4u& r, unsigned tag) { return (r[1] == tag) & (r[3] == tag); }

__global__ void __launch_bounds__(NTHR, 4)
decode_kernel(const unsigned short* __restrict__ x_, const unsigned short* __restrict__ k_,
              const unsigned short* __restrict__ v_, const unsigned short* __restrict__ wq_,
              const unsigned short* __restrict__ wo_, unsigned short* __restrict__ out_, unsigned char* ws_) {
  __shared__ __attribute__((aligned(16))) float lds[17 * DH + 9 * DH + 64 + 64 + 16];
  float* redo = lds;
  float* redw = lds + 17 * DH;
  unsigned* qs = reinterpret_cast<unsigned*>(lds + 26 * DH);
  float* wml = lds + 26 * DH + 64;
  LAS unsigned* lflag = (LAS unsigned*)(wml + 40);
  STAMP(0);
  asm volatile("" ::"s"(x_), "s"(k_), "s"(v_), "s"(wq_), "s"(wo_), "s"(out_), "s"(ws_));
  const int tid = threadIdx.x, lane = tid & 63;
  const int wv = __builtin_amdgcn_readfirstlane(tid >> 6);
  const int w = wv - 1;
  const int r = lane >> 4, c = lane & 15;
  const int b = blockIdx.x;
  const int h = b >> 3, s = b & 7;
  const int h2 = (b & 7) * 4 + ((b >> 3) & 3), c2 = b >> 5;
  constexpr float LOG2E = 1.4426950408889634f;
  const int nu = NU + ((h & BAL_MASK) == BAL_MASK && BAL_MASK >= 0 ? ((s & 1) ? -1 : 1) : 0);

  const __amdgpu_buffer_rsrc_t rs = __builtin_amdgcn_make_buffer_rsrc((void*)ws_, 0, WS_END, 0x00020000);

  if (__builtin_expect(wv == 0, 1)) {
    __builtin_amdgcn_s_setprio(3);
    const unsigned ep = __hip_atomic_load((GAS unsigned*)(ws_ + WS_EPOCH), __ATOMIC_RELAXED, __HIP_MEMORY_SCOPE_AGENT);
    const GAS v2u* xp = (const GAS v2u*)(x_ + QROWS_WG * s + QROWS_LANE * r);
    v2u xv[5];
#pragma unroll
    for (int i = 0; i < 5; ++i) xv[i] = xp[i];
    SB();
    const GAS v4u* wqp = (const GAS v4u*)(wq_ + (size_t)(QROWS_WG * s + QROWS_LANE * r) * (NH * DH) + h * DH + 8 * c);
    v4u wq[QROWS_LANE];
#pragma unroll
    for (int i = 0; i < QROWS_LANE; ++i) wq[i] = ld_stream(wqp + (size_t)i * (NH * DH / 8));
    SB();
    if (lane < 16) LDS_ST(lflag + lane, 0u);
    wg_barrier();
    float qa[8];
#pragma unroll
    for (int k = 0; k < 8; ++k) qa[k] = 0.f;
#pragma unroll
    for (int j = 0; j < QROWS_LANE / 2; ++j) {
      const unsigned xpair = xv[j >> 1][j & 1];
#pragma unroll
      for (int k = 0; k < 4; ++k) {
        const unsigned a = wq[2 * j][k], bb = wq[2 * j + 1][k];
        qa[2 * k] = dot2(__builtin_amdgcn_perm(bb, a, 0x05040100u), xpair, qa[2 * k]);
        qa[2 * k + 1] = dot2(__builtin_amdgcn_perm(bb, a, 0x07060302u), xpair, qa[2 * k + 1]);
      }
    }
    const unsigned tg = ep + 1u;
    float u[4], t[2];
#pragma unroll
    for (int k = 0; k < 4; ++k) u[k] = swap32_add(qa[k], qa[k + 4]);
#pragma unroll
    for (int k = 0; k < 2; ++k) t[k] = swap16_add(u[k], u[k + 2]);
    const v4u rec = {asu(t[0]), tg, asu(t[1]), tg};
    __builtin_amdgcn_raw_buffer_store_b128(rec, rs, WS_Q + (h * NSPLIT + s) * 1024 + (8 * c + 2 * r) * 8, 0, AUX_SC1);
    STAMP(1);
    v4u g[NSPLIT];
#pragma unroll
    for (int j = 0; j < NSPLIT; ++j) g[j] = ld_sc1(rs, WS_Q + (h * NSPLIT + j) * 1024 + lane * 16);
    for (unsigned spins = 0;; ++spins) {
      bool ok = true;
#pragma unroll
      for (int j = 0; j < NSPLIT; ++j) ok &= tagged(g[j], tg);
      if (__all(ok) || spins > SPIN_CAP) break;
      __builtin_amdgcn_s_sleep(1);
#pragma unroll
      for (int j = 0; j < NSPLIT; ++j)
        if (!tagged(g[j], tg)) g[j] = ld_sc1(rs, WS_Q + (h * NSPLIT + j) * 1024 + lane * 16);
    }
    float q0 = 0.f, q1 = 0.f;
#pragma unroll
    for (int j = 0; j < NSPLIT; ++j) { q0 += asf(g[j][0]); q1 += asf(g[j][2]); }
    qs[lane] = cvtpk(q0, q1);
    if (lane == 0) LDS_ST(lflag + 1, tg);
    LDS_ORDER();
    if (lane == 0) LDS_ST(lflag, 1u);
    STAMP(2);
    float M = -INFINITY, L = 0.f, o0 = 0.f, o1 = 0.f;
#pragma unroll 1
    for (int j = 0; j < nu; ++j) {
      for (unsigned spins = 0; spins < (SPIN_CAP << 4); ++spins)
        if ((__builtin_amdgcn_readfirstlane(LDS_LD(lflag + 3)) >> j) & 1u) break;
      asm volatile("" ::: "memory");
      const f32x2 ml = *reinterpret_cast<const f32x2*>(wml + 2 * j);
      const f32x2 ov = *reinterpret_cast<const f32x2*>(redo + j * DH + 2 * lane);
      const float Mn = fmaxf(M, ml[0]);
      const float a = __builtin_amdgcn_exp2f((M - Mn) * LOG2E), f = __builtin_amdgcn_exp2f((ml[0] - Mn) * LOG2E);
      o0 = fmaf(o0, a, f * ov[0]);
      o1 = fmaf(o1, a, f * ov[1]);
      L = fmaf(L, a, f * ml[1]);
      M = Mn;
      asm volatile("" : "+v"(o0), "+v"(o1), "+v"(L), "+v"(M));
    }
    const unsigned xw = asu((lane & 1) ? L : M);
    const v4u rec1 = {asu(o0), tg ^ mix(asu(o1), xw), asu(o1), xw};
    __builtin_amdgcn_raw_buffer_store_b128(rec1, rs, WS_O + (h * NSPLIT + s) * (NREC * 1024) + lane * 16, 0, AUX_SC1);
    if (lane == 0) LDS_ST(lflag + 5, 1u);
    STAMP(7);
    STAMP_OUT();
    return;
  }

  const size_t kvbase = ((size_t)h * SK + (SK - WIN)) * DH * 2;
  const GAS unsigned char* kp0 = (const GAS unsigned char*)k_ + kvbase;
  const GAS unsigned char* vp0 = (const GAS unsigned char*)v_ + kvbase;
  const unsigned loff = lane * 16;
  const int rot = h >> 2;
  v4u qp = {0u, 0u, 0u, 0u};
  unsigned tag = 0u;
  if (w >= AHEAD) wg_barrier();
  for (int j = w; j < nu; j += NKV) {
    const int jj = j < NU ? j : NU - 1, ss = j < NU ? s : s + 1;
    const int unit = ((jj + h) & (NU - 1)) * NSPLIT + ((ss + jj) & 7);
    if (j >= AHEAD) lds_wait_ge(lflag + 2, j - AHEAD + 1);
    if (j == w) STAMP(1);
    const GAS unsigned char* kp = kp0 + (size_t)unit * (UT * DH * 2);
    const GAS unsigned char* vp = vp0 + (size_t)unit * (UT * DH * 2);
    v4u kk[8], vv[8];
    SB();
#pragma unroll
    for (int i = 0; i < 8; ++i) kk[i] = ld_stream((const GAS v4u*)(kp + (((i + rot) & 7) << 10) + loff));
#pragma unroll
    for (int i = 0; i < 8; ++i) vv[i] = ld_stream((const GAS v4u*)(vp + (((i + rot) & 7) << 10) + loff));
    SB();
    if (j < AHEAD) wg_barrier();
    asm volatile("s_waitcnt vmcnt(" STR(TH_X) ")" ::: "memory");
    if (lane == 0) LDS_ADD(lflag + 2, 1u);
    SB();
    STAMP(3);
    if (j == w) {
      lds_wait_ge(lflag, 1);
      qp = *reinterpret_cast<const v4u*>(qs + 4 * c);
      tag = LDS_LD(lflag + 1);
      STAMP(4);
    }
    float sc[8];
#pragma unroll
    for (int i = 0; i < 8; ++i) {
      float d = 0.f;
#pragma unroll
      for (int k = 0; k < 4; ++k) d = dot2(kk[i][k], qp[k], d);
      sc[i] = d;
    }
#define TR_STEP(HALF, CTRL, BIT)                                  \
  _Pragma("unroll") for (int i = 0; i < (HALF); ++i) {            \
    const float lo_ = sc[i], hi_ = sc[i + (HALF)];                \
    const bool up_ = (c & (BIT)) != 0;                            \
    const float keep_ = up_ ? hi_ : lo_, send_ = up_ ? lo_ : hi_; \
    sc[i] = keep_ + dpp<CTRL>(send_);                             \
  }
    TR_STEP(4, XOR8, 8) TR_STEP(2, XOR7, 4) TR_STEP(1, XOR2, 2)
#undef TR_STEP
    const float sraw = sc[0] + dpp<XOR1>(sc[0]);
    const float sv = bf16r(bf16r(sraw) * 0.08838834764831845f);
    float mx = sv;
    mx = fmaxf(mx, dpp<XOR2>(mx));
    mx = fmaxf(mx, dpp<XOR7>(mx));
    mx = fmaxf(mx, dpp<XOR8>(mx));
    mx = swap16_max(mx);
    mx = swap32_max(mx);
    const float p = __builtin_amdgcn_exp2f((sv - mx) * LOG2E);
    float l = p;
    l += dpp<XOR2>(l);
    l += dpp<XOR7>(l);
    l += dpp<XOR8>(l);
    l = swap16_add(l, l);
    l = swap32_add(l, l);
    if (lane == 0) *reinterpret_cast<f32x2*>(wml + 2 * j) = f32x2{mx, l};
    float acc[8];
#pragma unroll
    for (int k = 0; k < 8; ++k) acc[k] = 0.f;
#define PV_STEP(I)                                                       \
  {                                                                      \
    const float pi_ = dpp<0x150 + 2 * (I)>(p);                           \
    _Pragma("unroll") for (int k = 0; k < 4; ++k) {                      \
      const unsigned u_ = vv[I][k];                                      \
      acc[2 * k] = fmaf(pi_, asf(u_ << 16), acc[2 * k]);                 \
      acc[2 * k + 1] = fmaf(pi_, asf(u_ & 0xffff0000u), acc[2 * k + 1]); \
    }                                                                    \
    asm volatile("" : "+v"(acc[0]), "+v"(acc[1]), "+v"(acc[2]), "+v"(acc[3]), "+v"(acc[4]), "+v"(acc[5]), "+v"(acc[6]), "+v"(acc[7])); \
  }
    PV_STEP(0) PV_STEP(1) PV_STEP(2) PV_STEP(3) PV_STEP(4) PV_STEP(5) PV_STEP(6) PV_STEP(7)
#undef PV_STEP
    STAMP(5);
    float u[4], t[2];
#pragma unroll
    for (int k = 0; k < 4; ++k) u[k] = swap32_add(acc[k], acc[k + 4]);
#pragma unroll
    for (int k = 0; k < 2; ++k) t[k] = swap16_add(u[k], u[k + 2]);
    *reinterpret_cast<f32x2*>(redo + j * DH + 8 * c + 2 * r) = f32x2{t[0], t[1]};
    LDS_ORDER();
    if (lane == 0) LDS_ADD(lflag + 3, 1u << j);
    STAMP(6);
  }
  if ((w >= 2 && w <= 9) || (DRY && w == 11)) {
    const bool dry = w == 11;
    const int tw = dry ? 8 : w - 2;
    const int ch = dry ? 0 : c < 10 ? c : 9;
    const GAS v4u* wop = (const GAS v4u*)(wo_ + (size_t)(h2 * DH + (dry ? 0 : 16 * tw + 4 * r)) * DM + c2 * CB + 8 * ch);
    lds_wait_ge(lflag + 2, dry ? 0 : nu);
    v4u wo[4];
#pragma unroll
    for (int jj = 0; jj < 4; ++jj) wo[jj] = ld_stream(wop + jj * (DM / 8));
    SB();
    const int sp = c & 7, pr = c >> 3;
    const int ooff = WS_O + (h2 * NSPLIT + sp) * (NREC * 1024) + (16 * tw + 4 * r + 2 * pr) * 8;
    unsigned p01[8], p23[8];
#pragma unroll
    for (int k = 0; k < 4; ++k) {
      p01[2 * k] = __builtin_amdgcn_perm(wo[1][k], wo[0][k], 0x05040100u);
      p01[2 * k + 1] = __builtin_amdgcn_perm(wo[1][k], wo[0][k], 0x07060302u);
      p23[2 * k] = __builtin_amdgcn_perm(wo[3][k], wo[2][k], 0x05040100u);
      p23[2 * k + 1] = __builtin_amdgcn_perm(wo[3][k], wo[2][k], 0x07060302u);
    }
    asm volatile("" : "+v"(p01[0]), "+v"(p01[1]), "+v"(p01[2]), "+v"(p01[3]), "+v"(p01[4]), "+v"(p01[5]), "+v"(p01[6]), "+v"(p01[7]));
    asm volatile("" : "+v"(p23[0]), "+v"(p23[1]), "+v"(p23[2]), "+v"(p23[3]), "+v"(p23[4]), "+v"(p23[5]), "+v"(p23[6]), "+v"(p23[7]));
    lds_wait_ge(lflag + 5, dry ? 0 : 1);
    unsigned bo0[NREC], bo1[NREC], bx[NREC];
    int go[NREC];
#pragma unroll
    for (int k = 0; k < NREC; ++k) { bo0[k] = 0u; bo1[k] = 0u; bx[k] = 0u; go[k] = dry ? OOB : ooff + k * 1024; }
    for (unsigned spins = 0; spins <= SPIN_CAP; ++spins) {
      v4u ra[NREC];
#pragma unroll
      for (int k = 0; k < NREC; ++k) { asm volatile("" : "+v"(go[k])); ra[k] = ld_sc1(rs, go[k]); }
      SB();
      bool all = true;
#pragma unroll
      for (int k = 0; k < NREC; ++k) {
        const bool to = ((ra[k][1] ^ mix(ra[k][2], ra[k][3])) == tag) & (go[k] != OOB);
        bo0[k] = to ? ra[k][0] : bo0[k]; bo1[k] = to ? ra[k][2] : bo1[k]; bx[k] = to ? ra[k][3] : bx[k];
        go[k] = to ? OOB : go[k];
        all &= go[k] == OOB;
      }
      if (__all(all)) break;
      __builtin_amdgcn_s_sleep(1);
    }
    if (!dry) STAMP(8);
    float ms[NREC], ls[NREC], Mh = -INFINITY;
#pragma unroll
    for (int k = 0; k < NREC; ++k) {
      const unsigned bxo = dppu<XOR8>(bx[k]);
      ms[k] = asf(pr ? bxo : bx[k]);
      ls[k] = asf(pr ? bx[k] : bxo);
      Mh = fmaxf(Mh, ms[k]);
    }
    Mh = fmaxf(Mh, dpp<XOR1>(Mh));
    Mh = fmaxf(Mh, dpp<XOR2>(Mh));
    Mh = fmaxf(Mh, dpp<XOR7>(Mh));
    float Lh = 0.f, o0 = 0.f, o1 = 0.f;
#pragma unroll
    for (int k = 0; k < NREC; ++k) {
      const float ws = __builtin_amdgcn_exp2f((ms[k] - Mh) * LOG2E);
      Lh = fmaf(ws, ls[k], Lh);
      o0 = fmaf(ws, asf(bo0[k]), o0);
      o1 = fmaf(ws, asf(bo1[k]), o1);
    }
    Lh += dpp<XOR1>(Lh); o0 += dpp<XOR1>(o0); o1 += dpp<XOR1>(o1);
    Lh += dpp<XOR2>(Lh); o0 += dpp<XOR2>(o0); o1 += dpp<XOR2>(o1);
    Lh += dpp<XOR7>(Lh); o0 += dpp<XOR7>(o0); o1 += dpp<XOR7>(o1);
    const float inv = __builtin_amdgcn_rcpf(Lh);
    const unsigned mine = cvtpk(o0 * inv, o1 * inv);
    const unsigned other = dppu<XOR8>(mine);
    const unsigned o01 = pr ? other : mine, o23 = pr ? mine : other;
    float g8[8];
#pragma unroll
    for (int k = 0; k < 8; ++k) g8[k] = dot2(p23[k], o23, dot2(p01[k], o01, 0.f));
    float u[4], t[2];
#pragma unroll
    for (int k = 0; k < 4; ++k) u[k] = swap32_add(g8[k], g8[k + 4]);
#pragma unroll
    for (int k = 0; k < 2; ++k) t[k] = swap16_add(u[k], u[k + 2]);
    *reinterpret_cast<f32x2*>(redw + tw * DH + 8 * c + 2 * r) = f32x2{t[0], t[1]};
    LDS_ORDER();
    unsigned old = 0u;
    if (lane == 0) old = LDS_ADD(lflag + (dry ? 6 : 4), 1u);
    old = __builtin_amdgcn_readfirstlane(old);
    asm volatile("" ::: "memory");
    if (old == (dry ? 0u : 7u) && lane < CB / 2) {
      f32x2 o = {0.f, 0.f};
#pragma unroll
      for (int ww = 0; ww < 8; ++ww) o += *reinterpret_cast<const f32x2*>(redw + ww * DH + 2 * lane);
      const v4u rec3 = {asu(o[0]), tag, asu(o[1]), tag};
      const int cg = c2 * (CB / 16) + (lane >> 3);
      __builtin_amdgcn_raw_buffer_store_b128(rec3, rs, dry ? OOB : WS_H2 + cg * 4096 + h2 * 128 + (lane & 7) * 16, 0, AUX_SC1);
    }
    if (!dry) STAMP(9);
  } else if ((w == 10 || (DRY && w == 12)) && h2 < CB / 16) {
    const bool dry = w == 12;
    const int cg = c2 * (CB / 16) + h2;
    const int goff = WS_H2 + cg * 4096 + lane * 16;
    lds_wait_ge(lflag + 4, dry ? 0 : 1);
    unsigned b0[4] = {0u, 0u, 0u, 0u}, b1[4] = {0u, 0u, 0u, 0u};
    int go[4];
#pragma unroll
    for (int k = 0; k < 4; ++k) go[k] = dry ? OOB : goff + k * 1024;
    for (unsigned spins = 0; spins <= SPIN_CAP; ++spins) {
      asm volatile("" : "+v"(go[0]), "+v"(go[1]), "+v"(go[2]), "+v"(go[3]));
      v4u ra[4];
#pragma unroll
      for (int k = 0; k < 4; ++k) ra[k] = ld_sc1(rs, go[k]);
      SB();
      bool all = true;
#pragma unroll
      for (int k = 0; k < 4; ++k) {
        const bool tv = tagged(ra[k], tag) & (go[k] != OOB);
        b0[k] = tv ? ra[k][0] : b0[k]; b1[k] = tv ? ra[k][2] : b1[k];
        go[k] = tv ? OOB : go[k];
        all &= go[k] == OOB;
      }
      if (__all(all)) break;
      __builtin_amdgcn_s_sleep(1);
    }
    float a0 = (asf(b0[0]) + asf(b0[1])) + (asf(b0[2]) + asf(b0[3]));
    float a1 = (asf(b1[0]) + asf(b1[1])) + (asf(b1[2]) + asf(b1[3]));
    a0 += dpp<XOR8>(a0); a1 += dpp<XOR8>(a1);
    a0 = swap16_add(a0, a0); a1 = swap16_add(a1, a1);
    a0 = swap32_add(a0, a0); a1 = swap32_add(a1, a1);
    const __amdgpu_buffer_rsrc_t ro = __builtin_amdgcn_make_buffer_rsrc((void*)out_, 0, DM * 2, 0x00020000);
    __builtin_amdgcn_raw_buffer_store_b32(cvtpk(a0, a1), ro, (lane < 8 && !dry) ? 32 * cg + 4 * lane : OOB, 0, 0);
    if (dry) {
      const unsigned junk = __builtin_amdgcn_raw_buffer_load_b32(ro, lane == 0 ? 32 * cg : OOB, 0, 0);
      asm volatile("" ::"v"(junk));
    }
    if (cg == 0 && lane == 0 && !dry)
      __hip_atomic_store((GAS unsigned*)(ws_ + WS_EPOCH), tag, __ATOMIC_RELAXED, __HIP_MEMORY_SCOPE_AGENT);
    if (!dry) STAMP(10);
  }
  STAMP_OUT();
}

}

extern "C" void kernel_launch(void* const* d_in, const int* in_sizes, int n_in, void* d_out, int out_size, void* d_ws,
                              size_t ws_size, hipStream_t stream) {
  (void)in_sizes; (void)n_in; (void)out_size; (void)ws_size;
  decode_kernel<<<dim3(NH * NSPLIT), dim3(NTHR), 0, stream>>>(
      (const unsigned short*)d_in[0], (const unsigned short*)d_in[1], (const unsigned short*)d_in[2],
      (const unsigned short*)d_in[3], (const unsigned short*)d_in[4], (unsigned short*)d_out, (unsigned char*)d_ws);
}
```

```cpp
#include <hip/hip_runtime.h>
#include <hip/hip_bf16.h>
#include <stdint.h>

#define GAS __attribute__((address_space(1)))
#define LAS __attribute__((address_space(3)))
typedef unsigned v4u __attribute__((ext_vector_type(4)));
typedef unsigned v2u __attribute__((ext_vector_type(2)));
typedef unsigned v8u __attribute__((ext_vector_type(8)));
typedef float f32x2 __attribute__((ext_vector_type(2)));
typedef __bf16 bf16x2 __attribute__((ext_vector_type(2)));

namespace {

constexpr int DM = 640, NH = 32, DH = 128, SK = 16384, WIN = 4096;
constexpr int NSPLIT = 8;
constexpr int NKV = 15;
constexpr int NTHR = (NKV + 1) * 64;
constexpr int UT = 32;
constexpr int NU = WIN / UT / NSPLIT;
constexpr int QROWS_WG = DM / NSPLIT;
constexpr int QROWS_LANE = QROWS_WG / 4;
constexpr int CB = DM / NSPLIT;
constexpr int AHEAD = 3;
constexpr int WS_EPOCH = 0;
constexpr int WS_O = 32768;
constexpr int WS_Q = WS_O + NH * NSPLIT * 1024;
constexpr int WS_H2 = WS_Q + NH * NSPLIT * 1024;
constexpr int WS_END = WS_H2 + 40 * 4096;
constexpr int OOB = 0x7ffffff0;
constexpr unsigned SPIN_CAP = 1u << 18;
constexpr int AUX_SC1 = 16;

__device__ __forceinline__ float asf(unsigned u) { return __builtin_bit_cast(float, u); }
__device__ __forceinline__ unsigned asu(float f) { return __builtin_bit_cast(unsigned, f); }
__device__ __forceinline__ float dot2(unsigned a, unsigned b, float acc) {
  return __builtin_amdgcn_fdot2_f32_bf16(__builtin_bit_cast(bf16x2, a), __builtin_bit_cast(bf16x2, b), acc, false);
}
template <int CTRL> __device__ __forceinline__ float dpp(float x) {
  return asf((unsigned)__builtin_amdgcn_mov_dpp((int)asu(x), CTRL, 0xf, 0xf, true));
}
template <int CTRL> __device__ __forceinline__ unsigned dppu(unsigned x) {
  return (unsigned)__builtin_amdgcn_mov_dpp((int)x, CTRL, 0xf, 0xf, true);
}
constexpr int XOR1 = 0xB1, XOR2 = 0x4E, XOR7 = 0x141, XOR8 = 0x128;
__device__ __forceinline__ unsigned cvtpk(float lo, float hi) {
  f32x2 v = {lo, hi};
  return __builtin_bit_cast(unsigned, __builtin_convertvector(v, bf16x2));
}
__device__ __forceinline__ float bf16r(float x) { return asf(cvtpk(x, x) & 0xffff0000u); }
__device__ __forceinline__ float swap32_add(float a, float b) {
  auto r = __builtin_amdgcn_permlane32_swap(asu(a), asu(b), false, false);
  return asf(r[0]) + asf(r[1]);
}
__device__ __forceinline__ float swap16_add(float a, float b) {
  auto r = __builtin_amdgcn_permlane16_swap(asu(a), asu(b), false, false);
  return asf(r[0]) + asf(r[1]);
}
__device__ __forceinline__ float swap32_max(float a) {
  auto r = __builtin_amdgcn_permlane32_swap(asu(a), asu(a), false, false);
  return fmaxf(asf(r[0]), asf(r[1]));
}
__device__ __forceinline__ float swap16_max(float a) {
  auto r = __builtin_amdgcn_permlane16_swap(asu(a), asu(a), false, false);
  return fmaxf(asf(r[0]), asf(r[1]));
}
__device__ __forceinline__ v4u ld_stream(const GAS v4u* p) { return __builtin_nontemporal_load(p); }
#define SB() __builtin_amdgcn_sched_barrier(0)
#define LDS_ST(p, v) __hip_atomic_store((p), (v), __ATOMIC_RELAXED, __HIP_MEMORY_SCOPE_WORKGROUP)
#define LDS_LD(p) __hip_atomic_load((p), __ATOMIC_RELAXED, __HIP_MEMORY_SCOPE_WORKGROUP)
#define LDS_ADD(p, v) __hip_atomic_fetch_add((p), (v), __ATOMIC_RELAXED, __HIP_MEMORY_SCOPE_WORKGROUP)
#define LDS_ORDER() asm volatile("" ::: "memory")
__device__ __forceinline__ void wg_barrier() {
  SB();
  asm volatile("s_waitcnt lgkmcnt(0)" ::: "memory");
  __builtin_amdgcn_s_barrier();
  asm volatile("" ::: "memory");
  SB();
}
__device__ __forceinline__ void lds_wait_ge(LAS unsigned* p, int want) {
#pragma nounroll
  for (unsigned spins = 0; spins < (SPIN_CAP << 4); ++spins) {
    if ((int)__builtin_amdgcn_readfirstlane(LDS_LD(p)) >= want) break;
    __builtin_amdgcn_s_sleep(1);
  }
  asm volatile("" ::: "memory");
}
__device__ __forceinline__ v4u ld_sc1(__amdgpu_buffer_rsrc_t rs, int off) {
  return __builtin_amdgcn_raw_buffer_load_b128(rs, off, 0, AUX_SC1);
}
__device__ __forceinline__ unsigned mix(unsigned b, unsigned x) {
  return __builtin_amdgcn_alignbit(b, b, 7) ^ __builtin_amdgcn_alignbit(x, x, 19);
}
__device__ __forceinline__ bool tagged(const v4u& r, unsigned tag) { return (r[1] == tag) & (r[3] == tag); }

__global__ void __launch_bounds__(NTHR, 4)
decode_kernel(const unsigned short* __restrict__, const unsigned short* __restrict__, const unsigned short* __restrict__,
              const unsigned short* __restrict__, const unsigned short* __restrict__, unsigned short* __restrict__,
              unsigned char*) {
  v8u ka0;
  v4u ka1;
  v2u ka2;
  asm volatile("s_load_dwordx8 %0, %3, 0x0\n\ts_load_dwordx4 %1, %3, 0x20\n\ts_load_dwordx2 %2, %3, 0x30"
               : "=&s"(ka0), "=&s"(ka1), "=&s"(ka2)
               : "s"(__builtin_amdgcn_kernarg_segment_ptr()));
#define KA_WAIT() asm volatile("s_waitcnt lgkmcnt(0)" : "+s"(ka0), "+s"(ka1), "+s"(ka2))
#define KA_PTR(T, lo, hi) ((T)(((unsigned long long)(hi) << 32) | (lo)))
#define x_ KA_PTR(const unsigned short*, ka0[0], ka0[1])
#define wq_ KA_PTR(const unsigned short*, ka0[6], ka0[7])
#define wo_ KA_PTR(const unsigned short*, ka1[0], ka1[1])
#define out_ KA_PTR(unsigned short*, ka1[2], ka1[3])
#define ws_ KA_PTR(unsigned char*, ka2[0], ka2[1])
  __shared__ __attribute__((aligned(16))) float lds[17 * DH + 9 * DH + 64 + 64 + 16];
  float* redo = lds;
  float* redw = lds + 17 * DH;
  unsigned* qs = reinterpret_cast<unsigned*>(lds + 26 * DH);
  float* wml = lds + 26 * DH + 64;
  LAS unsigned* lflag = (LAS unsigned*)(wml + 40);
  const int tid = threadIdx.x, lane = tid & 63;
  const int wv = __builtin_amdgcn_readfirstlane(tid >> 6);
  const int w = wv - 1;
  const int r = lane >> 4, c = lane & 15;
  const int b = blockIdx.x;
  const int h = b >> 3, s = b & 7;
  const int h2 = (b & 7) * 4 + ((b >> 3) & 3), c2 = b >> 5;
  constexpr float LOG2E = 1.4426950408889634f;
  const int nu = NU + ((h & 1) ? ((s & 1) ? -1 : 1) : 0);

  if (__builtin_expect(wv == 0, 1)) {
    __builtin_amdgcn_s_setprio(3);
    KA_WAIT();
    const __amdgpu_buffer_rsrc_t rs = __builtin_amdgcn_make_buffer_rsrc((void*)ws_, 0, WS_END, 0x00020000);
    const unsigned ep = __hip_atomic_load((GAS unsigned*)(ws_ + WS_EPOCH), __ATOMIC_RELAXED, __HIP_MEMORY_SCOPE_AGENT);
    const GAS v2u* xp = (const GAS v2u*)(x_ + QROWS_WG * s + QROWS_LANE * r);
    v2u xv[5];
#pragma unroll
    for (int i = 0; i < 5; ++i) xv[i] = xp[i];
    SB();
    const GAS v4u* wqp = (const GAS v4u*)(wq_ + (size_t)(QROWS_WG * s + QROWS_LANE * r) * (NH * DH) + h * DH + 8 * c);
    v4u wq[QROWS_LANE];
#pragma unroll
    for (int i = 0; i < QROWS_LANE; ++i) wq[i] = ld_stream(wqp + (size_t)i * (NH * DH / 8));
    SB();
    if (lane < 16) LDS_ST(lflag + lane, 0u);
    wg_barrier();
    float qa[8];
#pragma unroll
    for (int k = 0; k < 8; ++k) qa[k] = 0.f;
#pragma unroll
    for (int j = 0; j < QROWS_LANE / 2; ++j) {
      const unsigned xpair = xv[j >> 1][j & 1];
#pragma unroll
      for (int k = 0; k < 4; ++k) {
        const unsigned a = wq[2 * j][k], bb = wq[2 * j + 1][k];
        qa[2 * k] = dot2(__builtin_amdgcn_perm(bb, a, 0x05040100u), xpair, qa[2 * k]);
        qa[2 * k + 1] = dot2(__builtin_amdgcn_perm(bb, a, 0x07060302u), xpair, qa[2 * k + 1]);
      }
    }
    const unsigned tg = ep + 1u + (ep == 0xffffffffu);
    float u[4], t[2];
#pragma unroll
    for (int k = 0; k < 4; ++k) u[k] = swap32_add(qa[k], qa[k + 4]);
#pragma unroll
    for (int k = 0; k < 2; ++k) t[k] = swap16_add(u[k], u[k + 2]);
    const v4u rec = {asu(t[0]), tg, asu(t[1]), tg};
    __builtin_amdgcn_raw_buffer_store_b128(rec, rs, WS_Q + (h * NSPLIT + s) * 1024 + (8 * c + 2 * r) * 8, 0, AUX_SC1);
    v4u g[NSPLIT];
#pragma unroll
    for (int j = 0; j < NSPLIT; ++j) g[j] = ld_sc1(rs, WS_Q + (h * NSPLIT + j) * 1024 + lane * 16);
    for (unsigned spins = 0;; ++spins) {
      bool ok = true;
#pragma unroll
      for (int j = 0; j < NSPLIT; ++j) ok &= tagged(g[j], tg);
      if (__all(ok) || spins > SPIN_CAP) break;
      __builtin_amdgcn_s_sleep(1);
#pragma unroll
      for (int j = 0; j < NSPLIT; ++j)
        if (!tagged(g[j], tg)) g[j] = ld_sc1(rs, WS_Q + (h * NSPLIT + j) * 1024 + lane * 16);
    }
    float q0 = 0.f, q1 = 0.f;
#pragma unroll
    for (int j = 0; j < NSPLIT; ++j) { q0 += asf(g[j][0]); q1 += asf(g[j][2]); }
    qs[lane] = cvtpk(q0, q1);
    if (lane == 0) LDS_ST(lflag + 1, tg);
    LDS_ORDER();
    if (lane == 0) LDS_ST(lflag, 1u);
    float M = -INFINITY, L = 0.f, o0 = 0.f, o1 = 0.f;
#pragma unroll 1
    for (int j = 0; j < nu; ++j) {
      for (unsigned spins = 0; spins < (SPIN_CAP << 4); ++spins)
        if ((__builtin_amdgcn_readfirstlane(LDS_LD(lflag + 3)) >> j) & 1u) break;
      asm volatile("" ::: "memory");
      const f32x2 ml = *reinterpret_cast<const f32x2*>(wml + 2 * j);
      const f32x2 ov = *reinterpret_cast<const f32x2*>(redo + j * DH + 2 * lane);
      const float Mn = fmaxf(M, ml[0]);
      const float a = __builtin_amdgcn_exp2f((M - Mn) * LOG2E), f = __builtin_amdgcn_exp2f((ml[0] - Mn) * LOG2E);
      o0 = fmaf(o0, a, f * ov[0]);
      o1 = fmaf(o1, a, f * ov[1]);
      L = fmaf(L, a, f * ml[1]);
      M = Mn;
      asm volatile("" : "+v"(o0), "+v"(o1), "+v"(L), "+v"(M));
    }
    const unsigned xw = asu((lane & 1) ? L : M);
    const v4u rec1 = {asu(o0), tg ^ mix(asu(o1), xw), asu(o1), xw};
    __builtin_amdgcn_raw_buffer_store_b128(rec1, rs, WS_O + (h * NSPLIT + s) * 1024 + lane * 16, 0, AUX_SC1);
    if (lane == 0) LDS_ST(lflag + 5, 1u);
    return;
  }

  const size_t kvbase = ((size_t)h * SK + (SK - WIN)) * DH * 2;
  const unsigned loff = lane * 16;
  const int rot = h >> 2;
  v4u qp = {0u, 0u, 0u, 0u};
  unsigned tag = 0u;
  if (w >= AHEAD) wg_barrier();
  for (int j = w; j < nu; j += NKV) {
    const int jj = j < NU ? j : NU - 1, ss = j < NU ? s : s + 1;
    const int unit = ((jj + h) & (NU - 1)) * NSPLIT + ((ss + jj) & 7);
    if (j >= AHEAD) lds_wait_ge(lflag + 2, j - AHEAD + 1);
    size_t uoff = kvbase + (size_t)unit * (UT * DH * 2);
    asm volatile("" : "+s"(uoff));
    unsigned long long kb, vb;
    asm volatile("s_waitcnt lgkmcnt(0)\n\ts_mov_b64 %0, %2\n\ts_mov_b64 %1, %3"
                 : "=&s"(kb), "=&s"(vb)
                 : "s"(__builtin_shufflevector(ka0, ka0, 2, 3)), "s"(__builtin_shufflevector(ka0, ka0, 4, 5)));
    const GAS unsigned char* kp = (const GAS unsigned char*)kb + uoff;
    const GAS unsigned char* vp = (const GAS unsigned char*)vb + uoff;
    v4u kk[8], vv[8];
    SB();
#pragma unroll
    for (int i = 0; i < 8; ++i) kk[i] = ld_stream((const GAS v4u*)(kp + (((i + rot) & 7) << 10) + loff));
#pragma unroll
    for (int i = 0; i < 8; ++i) vv[i] = ld_stream((const GAS v4u*)(vp + (((i + rot) & 7) << 10) + loff));
    SB();
    if (j < AHEAD) wg_barrier();
    asm volatile("s_waitcnt vmcnt(12)" ::: "memory");
    if (lane == 0) LDS_ADD(lflag + 2, 1u);
    SB();
    if (j == w) {
      lds_wait_ge(lflag, 1);
      qp = *reinterpret_cast<const v4u*>(qs + 4 * c);
      tag = LDS_LD(lflag + 1);
    }
    float sc[8];
#pragma unroll
    for (int i = 0; i < 8; ++i) {
      float d = 0.f;
#pragma unroll
      for (int k = 0; k < 4; ++k) d = dot2(kk[i][k], qp[k], d);
      sc[i] = d;
    }
#define TR_STEP(HALF, CTRL, BIT)                                  \
  _Pragma("unroll") for (int i = 0; i < (HALF); ++i) {            \
    const float lo_ = sc[i], hi_ = sc[i + (HALF)];                \
    const bool up_ = (c & (BIT)) != 0;                            \
    const float keep_ = up_ ? hi_ : lo_, send_ = up_ ? lo_ : hi_; \
    sc[i] = keep_ + dpp<CTRL>(send_);                             \
  }
    TR_STEP(4, XOR8, 8) TR_STEP(2, XOR7, 4) TR_STEP(1, XOR2, 2)
#undef TR_STEP
    const float sraw = sc[0] + dpp<XOR1>(sc[0]);
    const float sv = bf16r(bf16r(sraw) * 0.08838834764831845f);
    float mx = sv;
    mx = fmaxf(mx, dpp<XOR2>(mx));
    mx = fmaxf(mx, dpp<XOR7>(mx));
    mx = fmaxf(mx, dpp<XOR8>(mx));
    mx = swap16_max(mx);
    mx = swap32_max(mx);
    const float p = __builtin_amdgcn_exp2f((sv - mx) * LOG2E);
    float l = p;
    l += dpp<XOR2>(l);
    l += dpp<XOR7>(l);
    l += dpp<XOR8>(l);
    l = swap16_add(l, l);
    l = swap32_add(l, l);
    if (lane == 0) *reinterpret_cast<f32x2*>(wml + 2 * j) = f32x2{mx, l};
    float acc[8];
#pragma unroll
    for (int k = 0; k < 8; ++k) acc[k] = 0.f;
#define PV_STEP(I)                                                       \
  {                                                                      \
    const float pi_ = dpp<0x150 + 2 * (I)>(p);          \
    _Pragma("unroll") for (int k = 0; k < 4; ++k) {                      \
      const unsigned u_ = vv[I][k];                                      \
      acc[2 * k] = fmaf(pi_, asf(u_ << 16), acc[2 * k]);                 \
      acc[2 * k + 1] = fmaf(pi_, asf(u_ & 0xffff0000u), acc[2 * k + 1]); \
    }                                                                    \
    asm volatile("" : "+v"(acc[0]), "+v"(acc[1]), "+v"(acc[2]), "+v"(acc[3]), "+v"(acc[4]), "+v"(acc[5]), "+v"(acc[6]), "+v"(acc[7])); \
  }
    PV_STEP(0) PV_STEP(1) PV_STEP(2) PV_STEP(3) PV_STEP(4) PV_STEP(5) PV_STEP(6) PV_STEP(7)
#undef PV_STEP
    float u[4], t[2];
#pragma unroll
    for (int k = 0; k < 4; ++k) u[k] = swap32_add(acc[k], acc[k + 4]);
#pragma unroll
    for (int k = 0; k < 2; ++k) t[k] = swap16_add(u[k], u[k + 2]);
    *reinterpret_cast<f32x2*>(redo + j * DH + 8 * c + 2 * r) = f32x2{t[0], t[1]};
    LDS_ORDER();
    if (lane == 0) LDS_ADD(lflag + 3, 1u << j);
  }
  KA_WAIT();
  const __amdgpu_buffer_rsrc_t rs = __builtin_amdgcn_make_buffer_rsrc((void*)ws_, 0, WS_END, 0x00020000);
  if ((w >= 2 && w <= 9) || w == 11) {
    const bool dry = w == 11;
    const int tw = dry ? 8 : w - 2;
    const int ch = dry ? 0 : c < 10 ? c : 9;
    const GAS v4u* wop = (const GAS v4u*)(wo_ + (size_t)(h2 * DH + (dry ? 0 : 16 * tw + 4 * r)) * DM + c2 * CB + 8 * ch);
    lds_wait_ge(lflag + 2, dry ? 0 : nu);
    v4u wo[4];
#pragma unroll
    for (int jj = 0; jj < 4; ++jj) wo[jj] = ld_stream(wop + jj * (DM / 8));
    SB();
    const int sp = c & 7, pr = c >> 3;
    const int ooff = WS_O + (h2 * NSPLIT + sp) * 1024 + (16 * tw + 4 * r + 2 * pr) * 8;
    unsigned p01[8], p23[8];
#pragma unroll
    for (int k = 0; k < 4; ++k) {
      p01[2 * k] = __builtin_amdgcn_perm(wo[1][k], wo[0][k], 0x05040100u);
      p01[2 * k + 1] = __builtin_amdgcn_perm(wo[1][k], wo[0][k], 0x07060302u);
      p23[2 * k] = __builtin_amdgcn_perm(wo[3][k], wo[2][k], 0x05040100u);
      p23[2 * k + 1] = __builtin_amdgcn_perm(wo[3][k], wo[2][k], 0x07060302u);
    }
    asm volatile("" : "+v"(p01[0]), "+v"(p01[1]), "+v"(p01[2]), "+v"(p01[3]), "+v"(p01[4]), "+v"(p01[5]), "+v"(p01[6]), "+v"(p01[7]));
    asm volatile("" : "+v"(p23[0]), "+v"(p23[1]), "+v"(p23[2]), "+v"(p23[3]), "+v"(p23[4]), "+v"(p23[5]), "+v"(p23[6]), "+v"(p23[7]));
    lds_wait_ge(lflag + 5, dry ? 0 : 1);
    unsigned bo0 = 0u, bo1 = 0u, bx = 0u;
    int go = dry ? OOB : ooff;
    for (unsigned spins = 0; spins <= SPIN_CAP; ++spins) {
      asm volatile("" : "+v"(go));
      const v4u ra = ld_sc1(rs, go);
      const bool to = (ra[1] ^ mix(ra[2], ra[3])) == tag;
      bo0 = to ? ra[0] : bo0; bo1 = to ? ra[2] : bo1; bx = to ? ra[3] : bx;
      go = to ? OOB : go;
      if (__builtin_amdgcn_ballot_w64(go != OOB) == 0ull) break;
      __builtin_amdgcn_s_sleep(1);
    }
    const unsigned bxo = dppu<XOR8>(bx);
    const float Ms = asf(pr ? bxo : bx), Ls = asf(pr ? bx : bxo);
    float Mh = Ms;
    Mh = fmaxf(Mh, dpp<XOR1>(Mh));
    Mh = fmaxf(Mh, dpp<XOR2>(Mh));
    Mh = fmaxf(Mh, dpp<XOR7>(Mh));
    const float ws = __builtin_amdgcn_exp2f((Ms - Mh) * LOG2E);
    float Lh = ws * Ls, o0 = ws * asf(bo0), o1 = ws * asf(bo1);
    Lh += dpp<XOR1>(Lh); o0 += dpp<XOR1>(o0); o1 += dpp<XOR1>(o1);
    Lh += dpp<XOR2>(Lh); o0 += dpp<XOR2>(o0); o1 += dpp<XOR2>(o1);
    Lh += dpp<XOR7>(Lh); o0 += dpp<XOR7>(o0); o1 += dpp<XOR7>(o1);
    const float inv = __builtin_amdgcn_rcpf(Lh);
    const unsigned mine = cvtpk(o0 * inv, o1 * inv);
    const unsigned other = dppu<XOR8>(mine);
    const unsigned o01 = pr ? other : mine, o23 = pr ? mine : other;
    float g8[8];
#pragma unroll
    for (int k = 0; k < 8; ++k) g8[k] = dot2(p23[k], o23, dot2(p01[k], o01, 0.f));
    float u[4], t[2];
#pragma unroll
    for (int k = 0; k < 4; ++k) u[k] = swap32_add(g8[k], g8[k + 4]);
#pragma unroll
    for (int k = 0; k < 2; ++k) t[k] = swap16_add(u[k], u[k + 2]);
    *reinterpret_cast<f32x2*>(redw + tw * DH + 8 * c + 2 * r) = f32x2{t[0], t[1]};
    LDS_ORDER();
    unsigned old = 0u;
    if (lane == 0) old = LDS_ADD(lflag + (dry ? 6 : 4), 1u);
    old = __builtin_amdgcn_readfirstlane(old);
    asm volatile("" ::: "memory");
    if (old == (dry ? 0u : 7u) && lane < CB / 2) {
      f32x2 o = {0.f, 0.f};
#pragma unroll
      for (int ww = 0; ww < 8; ++ww) o += *reinterpret_cast<const f32x2*>(redw + ww * DH + 2 * lane);
      const v4u rec3 = {asu(o[0]), tag, asu(o[1]), tag};
      const int cg = c2 * (CB / 16) + (lane >> 3);
      __builtin_amdgcn_raw_buffer_store_b128(rec3, rs, dry ? OOB : WS_H2 + cg * 4096 + h2 * 128 + (lane & 7) * 16, 0, AUX_SC1);
    }
  } else if ((w == 10 || w == 12) && h2 < CB / 4) {
    const bool dry = w == 12;
    const int cg = c2 * (CB / 4) + h2;
    lds_wait_ge(lflag + 4, dry ? 0 : 1);
    unsigned b0 = 0u, b1 = 0u;
    int go = dry ? OOB : WS_H2 + (cg >> 2) * 4096 + (lane & 31) * 128 + (cg & 3) * 32 + (lane >> 5) * 16;
    for (unsigned spins = 0; spins <= SPIN_CAP; ++spins) {
      asm volatile("" : "+v"(go));
      const v4u ra = ld_sc1(rs, go);
      const bool tv = tagged(ra, tag);
      b0 = tv ? ra[0] : b0; b1 = tv ? ra[2] : b1;
      go = tv ? OOB : go;
      if (__builtin_amdgcn_ballot_w64(go != OOB) == 0ull) break;
    }
    float a0 = asf(b0), a1 = asf(b1);
    a0 += dpp<XOR1>(a0); a1 += dpp<XOR1>(a1);
    a0 += dpp<XOR2>(a0); a1 += dpp<XOR2>(a1);
    a0 += dpp<XOR7>(a0); a1 += dpp<XOR7>(a1);
    a0 += dpp<XOR8>(a0); a1 += dpp<XOR8>(a1);
    a0 = swap16_add(a0, a0); a1 = swap16_add(a1, a1);
    const __amdgpu_buffer_rsrc_t ro = __builtin_amdgcn_make_buffer_rsrc((void*)out_, 0, DM * 2, 0x00020000);
    __builtin_amdgcn_raw_buffer_store_b32(cvtpk(a0, a1), ro, ((lane & 31) == 0 && !dry) ? 8 * cg + 4 * (lane >> 5) : OOB, 0, 0);
    if (dry) {
      const unsigned junk = __builtin_amdgcn_raw_buffer_load_b32(ro, lane == 0 ? 8 * cg : OOB, 0, 0);
      asm volatile("" ::"v"(junk));
    }
    if (cg == 0 && lane == 0 && !dry)
      __hip_atomic_store((GAS unsigned*)(ws_ + WS_EPOCH), tag, __ATOMIC_RELAXED, __HIP_MEMORY_SCOPE_AGENT);
  }
}

}

extern "C" void kernel_launch(void* const* d_in, const int* in_sizes, int n_in, void* d_out, int out_size, void* d_ws,
                              size_t ws_size, hipStream_t stream) {
  (void)in_sizes; (void)n_in; (void)out_size; (void)ws_size;
  decode_kernel<<<dim3(NH * NSPLIT), dim3(NTHR), 0, stream>>>(
      (const unsigned short*)d_in[0], (const unsigned short*)d_in[1], (const unsigned short*)d_in[2],
      (const unsigned short*)d_in[3], (const unsigned short*)d_in[4], (unsigned short*)d_out, (unsigned char*)d_ws);
}
```

```cpp
#include <hip/hip_runtime.h>
#include <hip/hip_bf16.h>
#include <stdint.h>

#define GAS __attribute__((address_space(1)))
#define LAS __attribute__((address_space(3)))
typedef unsigned v4u __attribute__((ext_vector_type(4)));
typedef unsigned v2u __attribute__((ext_vector_type(2)));
typedef unsigned v8u __attribute__((ext_vector_type(8)));
typedef float f32x2 __attribute__((ext_vector_type(2)));
typedef float f32x4 __attribute__((ext_vector_type(4)));
typedef __bf16 bf16x2 __attribute__((ext_vector_type(2)));

namespace {

constexpr int DM = 640, NH = 32, DH = 128, SK = 16384, WIN = 4096;
constexpr int NSPLIT = 8;
constexpr int NKV = 15;
constexpr int NTHR = (NKV + 1) * 64;
constexpr int UT = 32;
constexpr int NU = WIN / UT / NSPLIT;
constexpr int QROWS_WG = DM / NSPLIT;
constexpr int QROWS_LANE = QROWS_WG / 4;
constexpr int CB = DM / NSPLIT;
constexpr int NREC = 1;
constexpr int WS_EPOCH = 0;
constexpr int WS_O = 32768;
constexpr int WS_Q = WS_O + NH * NSPLIT * NREC * 1024;
constexpr int WS_H2 = WS_Q + NH * NSPLIT * 1024;
constexpr int WS_END = WS_H2 + 40 * 4096;
constexpr int OOB = 0x7ffffff0;
constexpr unsigned SPIN_CAP = 1u << 18;
constexpr int AUX_SC1 = 16;

#define TH_X 12
#define AHEAD 3
#define WO_TURN 16
#define CODE_PF 0
#define BAL_MASK 1
#define GATE1 1
#define MAPV 3
#define WSLP 1
#define XMAP 0
#define SLP2 0
#define GATE2W 4
#define SLP1 1
#define DRY 1
#define LDS_WAIT 0
#define STR2(x) #x
#define STR(x) STR2(x)

__device__ __forceinline__ float asf(unsigned u) { return __builtin_bit_cast(float, u); }
__device__ __forceinline__ unsigned asu(float f) { return __builtin_bit_cast(unsigned, f); }
__device__ __forceinline__ float dot2(unsigned a, unsigned b, float acc) {
  return __builtin_amdgcn_fdot2_f32_bf16(__builtin_bit_cast(bf16x2, a), __builtin_bit_cast(bf16x2, b), acc, false);
}
template <int CTRL> __device__ __forceinline__ float dpp(float x) {
  return asf((unsigned)__builtin_amdgcn_mov_dpp((int)asu(x), CTRL, 0xf, 0xf, true));
}
template <int CTRL> __device__ __forceinline__ unsigned dppu(unsigned x) {
  return (unsigned)__builtin_amdgcn_mov_dpp((int)x, CTRL, 0xf, 0xf, true);
}
constexpr int XOR1 = 0xB1, XOR2 = 0x4E, XOR7 = 0x141, XOR8 = 0x128;
__device__ __forceinline__ unsigned cvtpk(float lo, float hi) {
  f32x2 v = {lo, hi};
  return __builtin_bit_cast(unsigned, __builtin_convertvector(v, bf16x2));
}
__device__ __forceinline__ float bf16r(float x) { return asf(cvtpk(x, x) & 0xffff0000u); }
__device__ __forceinline__ float swap32_add(float a, float b) {
  auto r = __builtin_amdgcn_permlane32_swap(asu(a), asu(b), false, false);
  return asf(r[0]) + asf(r[1]);
}
__device__ __forceinline__ float swap16_add(float a, float b) {
  auto r = __builtin_amdgcn_permlane16_swap(asu(a), asu(b), false, false);
  return asf(r[0]) + asf(r[1]);
}
__device__ __forceinline__ float swap32_max(float a) {
  auto r = __builtin_amdgcn_permlane32_swap(asu(a), asu(a), false, false);
  return fmaxf(asf(r[0]), asf(r[1]));
}
__device__ __forceinline__ float swap16_max(float a) {
  auto r = __builtin_amdgcn_permlane16_swap(asu(a), asu(a), false, false);
  return fmaxf(asf(r[0]), asf(r[1]));
}
__device__ __forceinline__ v4u ld_stream(const GAS v4u* p) { return __builtin_nontemporal_load(p); }
#define SB() __builtin_amdgcn_sched_barrier(0)
#define STAMP(n)
#define STAMP_OUT()
#define LDS_ST(p, v) __hip_atomic_store((p), (v), __ATOMIC_RELAXED, __HIP_MEMORY_SCOPE_WORKGROUP)
#define LDS_LD(p) __hip_atomic_load((p), __ATOMIC_RELAXED, __HIP_MEMORY_SCOPE_WORKGROUP)
#define LDS_ADD(p, v) __hip_atomic_fetch_add((p), (v), __ATOMIC_RELAXED, __HIP_MEMORY_SCOPE_WORKGROUP)
#define LDS_ORDER() asm volatile("" ::: "memory")
__device__ __forceinline__ void wg_barrier() {
  SB();
  asm volatile("s_waitcnt lgkmcnt(0)" ::: "memory");
  __builtin_amdgcn_s_barrier();
  asm volatile("" ::: "memory");
  SB();
}
__device__ __forceinline__ void lds_wait_ge(LAS unsigned* p, int want) {
#pragma nounroll
  for (unsigned spins = 0; spins < (SPIN_CAP << 4); ++spins) {
    if ((int)__builtin_amdgcn_readfirstlane(LDS_LD(p)) >= want) break;
    __builtin_amdgcn_s_sleep(WSLP);
  }
  asm volatile("" ::: "memory");
}
__device__ __forceinline__ v4u ld_sc1(__amdgpu_buffer_rsrc_t rs, int off) {
  return __builtin_amdgcn_raw_buffer_load_b128(rs, off, 0, AUX_SC1);
}
__device__ __forceinline__ unsigned mix(unsigned b, unsigned x) {
  return __builtin_amdgcn_alignbit(b, b, 7) ^ __builtin_amdgcn_alignbit(x, x, 19);
}
__device__ __forceinline__ bool tagged(const v4u& r, unsigned tag) { return (r[1] == tag) & (r[3] == tag); }

__global__ void __launch_bounds__(NTHR, 4)
decode_kernel(const unsigned short* __restrict__, const unsigned short* __restrict__, const unsigned short* __restrict__,
              const unsigned short* __restrict__, const unsigned short* __restrict__, unsigned short* __restrict__,
              unsigned char*) {
  v8u ka0;
  v4u ka1;
  v2u ka2;
  asm volatile("s_load_dwordx8 %0, %3, 0x0\n\ts_load_dwordx4 %1, %3, 0x20\n\ts_load_dwordx2 %2, %3, 0x30"
               : "=&s"(ka0), "=&s"(ka1), "=&s"(ka2)
               : "s"(__builtin_amdgcn_kernarg_segment_ptr()));
#define KA_WAIT() asm volatile("s_waitcnt lgkmcnt(0)" : "+s"(ka0), "+s"(ka1), "+s"(ka2))
#define KA_PTR(T, lo, hi) ((T)(((unsigned long long)(hi) << 32) | (lo)))
#define x_ KA_PTR(const unsigned short*, ka0[0], ka0[1])
#define k_ KA_PTR(const unsigned short*, ka0[2], ka0[3])
#define v_ KA_PTR(const unsigned short*, ka0[4], ka0[5])
#define wq_ KA_PTR(const unsigned short*, ka0[6], ka0[7])
#define wo_ KA_PTR(const unsigned short*, ka1[0], ka1[1])
#define out_ KA_PTR(unsigned short*, ka1[2], ka1[3])
#define ws_ KA_PTR(unsigned char*, ka2[0], ka2[1])
  __shared__ __attribute__((aligned(16))) float lds[17 * DH + 9 * DH + 64 + 64 + 16];
  float* redo = lds;
  float* redw = lds + 17 * DH;
  unsigned* qs = reinterpret_cast<unsigned*>(lds + 26 * DH);
  float* wml = lds + 26 * DH + 64;
  LAS unsigned* lflag = (LAS unsigned*)(wml + 40);
  STAMP(0);
  const int tid = threadIdx.x, lane = tid & 63;
  const int wv = __builtin_amdgcn_readfirstlane(tid >> 6);
  const int w = wv - 1;
  const int r = lane >> 4, c = lane & 15;
  const int b = blockIdx.x;
  const int h = b >> 3, s = b & 7;
  const int h2 = (b & 7) * 4 + ((b >> 3) & 3), c2 = b >> 5;
  constexpr float LOG2E = 1.4426950408889634f;
  const int nu = NU + ((h & BAL_MASK) == BAL_MASK && BAL_MASK >= 0 ? ((s & 1) ? -1 : 1) : 0);

  if (__builtin_expect(wv == 0, 1)) {
    __builtin_amdgcn_s_setprio(3);
    KA_WAIT();
    const __amdgpu_buffer_rsrc_t rs = __builtin_amdgcn_make_buffer_rsrc((void*)ws_, 0, WS_END, 0x00020000);
    const unsigned ep = __hip_atomic_load((GAS unsigned*)(ws_ + WS_EPOCH), __ATOMIC_RELAXED, __HIP_MEMORY_SCOPE_AGENT);
    const GAS v2u* xp = (const GAS v2u*)(x_ + QROWS_WG * s + QROWS_LANE * r);
    v2u xv[5];
#pragma unroll
    for (int i = 0; i < 5; ++i) xv[i] = xp[i];
    SB();
    const GAS v4u* wqp = (const GAS v4u*)(wq_ + (size_t)(QROWS_WG * s + QROWS_LANE * r) * (NH * DH) + h * DH + 8 * c);
    v4u wq[QROWS_LANE];
#pragma unroll
    for (int i = 0; i < QROWS_LANE; ++i) wq[i] = ld_stream(wqp + (size_t)i * (NH * DH / 8));
    SB();
    if (lane < 16) LDS_ST(lflag + lane, 0u);
    wg_barrier();
    float qa[8];
#pragma unroll
    for (int k = 0; k < 8; ++k) qa[k] = 0.f;
#pragma unroll
    for (int j = 0; j < QROWS_LANE / 2; ++j) {
      const unsigned xpair = xv[j >> 1][j & 1];
#pragma unroll
      for (int k = 0; k < 4; ++k) {
        const unsigned a = wq[2 * j][k], bb = wq[2 * j + 1][k];
        qa[2 * k] = dot2(__builtin_amdgcn_perm(bb, a, 0x05040100u), xpair, qa[2 * k]);
        qa[2 * k + 1] = dot2(__builtin_amdgcn_perm(bb, a, 0x07060302u), xpair, qa[2 * k + 1]);
      }
    }
    const unsigned tg = ep + 1u + (ep == 0xffffffffu);
    float u[4], t[2];
#pragma unroll
    for (int k = 0; k < 4; ++k) u[k] = swap32_add(qa[k], qa[k + 4]);
#pragma unroll
    for (int k = 0; k < 2; ++k) t[k] = swap16_add(u[k], u[k + 2]);
    const v4u rec = {asu(t[0]), tg, asu(t[1]), tg};
    __builtin_amdgcn_raw_buffer_store_b128(rec, rs, WS_Q + (h * NSPLIT + s) * 1024 + (8 * c + 2 * r) * 8, 0, AUX_SC1);
    STAMP(1);
    v4u g[NSPLIT];
#pragma unroll
    for (int j = 0; j < NSPLIT; ++j) g[j] = ld_sc1(rs, WS_Q + (h * NSPLIT + j) * 1024 + lane * 16);
    for (unsigned spins = 0;; ++spins) {
      bool ok = true;
#pragma unroll
      for (int j = 0; j < NSPLIT; ++j) ok &= tagged(g[j], tg);
      if (__all(ok) || spins > SPIN_CAP) break;
      __builtin_amdgcn_s_sleep(1);
#pragma unroll
      for (int j = 0; j < NSPLIT; ++j)
        if (!tagged(g[j], tg)) g[j] = ld_sc1(rs, WS_Q + (h * NSPLIT + j) * 1024 + lane * 16);
    }
    float q0 = 0.f, q1 = 0.f;
#pragma unroll
    for (int j = 0; j < NSPLIT; ++j) { q0 += asf(g[j][0]); q1 += asf(g[j][2]); }
    qs[lane] = cvtpk(q0, q1);
    if (lane == 0) LDS_ST(lflag + 1, tg);
    LDS_ORDER();
    if (lane == 0) LDS_ST(lflag, 1u);
    STAMP(2);
    float M = -INFINITY, L = 0.f, o0 = 0.f, o1 = 0.f;
#pragma unroll 1
    for (int j = 0; j < nu; ++j) {
      for (unsigned spins = 0; spins < (SPIN_CAP << 4); ++spins)
        if ((__builtin_amdgcn_readfirstlane(LDS_LD(lflag + 3)) >> j) & 1u) break;
      asm volatile("" ::: "memory");
      const f32x2 ml = *reinterpret_cast<const f32x2*>(wml + 2 * j);
      const f32x2 ov = *reinterpret_cast<const f32x2*>(redo + j * DH + 2 * lane);
      const float Mn = fmaxf(M, ml[0]);
      const float a = __builtin_amdgcn_exp2f((M - Mn) * LOG2E), f = __builtin_amdgcn_exp2f((ml[0] - Mn) * LOG2E);
      o0 = fmaf(o0, a, f * ov[0]);
      o1 = fmaf(o1, a, f * ov[1]);
      L = fmaf(L, a, f * ml[1]);
      M = Mn;
      asm volatile("" : "+v"(o0), "+v"(o1), "+v"(L), "+v"(M));
    }
    const unsigned xw = asu((lane & 1) ? L : M);
    const v4u rec1 = {asu(o0), tg ^ mix(asu(o1), xw), asu(o1), xw};
    __builtin_amdgcn_raw_buffer_store_b128(rec1, rs, WS_O + (h * NSPLIT + s) * (NREC * 1024) + lane * 16, 0, AUX_SC1);
    if (lane == 0) LDS_ST(lflag + 5, 1u);
    STAMP(7);
    STAMP_OUT();
    return;
  }

  const size_t kvbase = ((size_t)h * SK + (SK - WIN)) * DH * 2;
  const unsigned loff = lane * 16;
  const int rot = h >> 2;
  v4u qp = {0u, 0u, 0u, 0u};
  unsigned tag = 0u;
  if (w >= AHEAD) wg_barrier();
  for (int j = w; j < nu; j += NKV) {
    const int jj = j < NU ? j : NU - 1, ss = j < NU ? s : s + 1;
    const int unit = ((jj + h) & (NU - 1)) * NSPLIT + ((ss + jj) & 7);
    if (j >= AHEAD) lds_wait_ge(lflag + 2, j - AHEAD + 1);
    if (j == w) STAMP(1);
    size_t uoff = kvbase + (size_t)unit * (UT * DH * 2);
    asm volatile("" : "+s"(uoff));
    unsigned long long kb, vb;
    asm volatile("s_waitcnt lgkmcnt(0)\n\ts_mov_b64 %0, %2\n\ts_mov_b64 %1, %3"
                 : "=&s"(kb), "=&s"(vb)
                 : "s"(__builtin_shufflevector(ka0, ka0, 2, 3)), "s"(__builtin_shufflevector(ka0, ka0, 4, 5)));
    const GAS unsigned char* kp = (const GAS unsigned char*)kb + uoff;
    const GAS unsigned char* vp = (const GAS unsigned char*)vb + uoff;
    v4u kk[8], vv[8];
    SB();
#pragma unroll
    for (int i = 0; i < 8; ++i) kk[i] = ld_stream((const GAS v4u*)(kp + (((i + rot) & 7) << 10) + loff));
#pragma unroll
    for (int i = 0; i < 8; ++i) vv[i] = ld_stream((const GAS v4u*)(vp + (((i + rot) & 7) << 10) + loff));
    SB();
    if (j < AHEAD) wg_barrier();
    asm volatile("s_waitcnt vmcnt(" STR(TH_X) ")" ::: "memory");
    if (lane == 0) LDS_ADD(lflag + 2, 1u);
    SB();
    STAMP(3);
    if (j == w) {
      lds_wait_ge(lflag, 1);
      qp = *reinterpret_cast<const v4u*>(qs + 4 * c);
      tag = LDS_LD(lflag + 1);
      STAMP(4);
    }
    float sc[8];
#pragma unroll
    for (int i = 0; i < 8; ++i) {
      float d = 0.f;
#pragma unroll
      for (int k = 0; k < 4; ++k) d = dot2(kk[i][k], qp[k], d);
      sc[i] = d;
    }
#define TR_STEP(HALF, CTRL, BIT)                                  \
  _Pragma("unroll") for (int i = 0; i < (HALF); ++i) {            \
    const float lo_ = sc[i], hi_ = sc[i + (HALF)];                \
    const bool up_ = (c & (BIT)) != 0;                            \
    const float keep_ = up_ ? hi_ : lo_, send_ = up_ ? lo_ : hi_; \
    sc[i] = keep_ + dpp<CTRL>(send_);                             \
  }
    TR_STEP(4, XOR8, 8) TR_STEP(2, XOR7, 4) TR_STEP(1, XOR2, 2)
#undef TR_STEP
    const float sraw = sc[0] + dpp<XOR1>(sc[0]);
    const float sv = bf16r(bf16r(sraw) * 0.08838834764831845f);
    float mx = sv;
    mx = fmaxf(mx, dpp<XOR2>(mx));
    mx = fmaxf(mx, dpp<XOR7>(mx));
    mx = fmaxf(mx, dpp<XOR8>(mx));
    mx = swap16_max(mx);
    mx = swap32_max(mx);
    const float p = __builtin_amdgcn_exp2f((sv - mx) * LOG2E);
    float l = p;
    l += dpp<XOR2>(l);
    l += dpp<XOR7>(l);
    l += dpp<XOR8>(l);
    l = swap16_add(l, l);
    l = swap32_add(l, l);
    if (lane == 0) *reinterpret_cast<f32x2*>(wml + 2 * j) = f32x2{mx, l};
    float acc[8];
#pragma unroll
    for (int k = 0; k < 8; ++k) acc[k] = 0.f;
#define PV_STEP(I)                                                       \
  {                                                                      \
    const float pi_ = dpp<0x150 + 2 * (I)>(p);                           \
    _Pragma("unroll") for (int k = 0; k < 4; ++k) {                      \
      const unsigned u_ = vv[I][k];                                      \
      acc[2 * k] = fmaf(pi_, asf(u_ << 16), acc[2 * k]);                 \
      acc[2 * k + 1] = fmaf(pi_, asf(u_ & 0xffff0000u), acc[2 * k + 1]); \
    }                                                                    \
    asm volatile("" : "+v"(acc[0]), "+v"(acc[1]), "+v"(acc[2]), "+v"(acc[3]), "+v"(acc[4]), "+v"(acc[5]), "+v"(acc[6]), "+v"(acc[7])); \
  }
    PV_STEP(0) PV_STEP(1) PV_STEP(2) PV_STEP(3) PV_STEP(4) PV_STEP(5) PV_STEP(6) PV_STEP(7)
#undef PV_STEP
    STAMP(5);
    float u[4], t[2];
#pragma unroll
    for (int k = 0; k < 4; ++k) u[k] = swap32_add(acc[k], acc[k + 4]);
#pragma unroll
    for (int k = 0; k < 2; ++k) t[k] = swap16_add(u[k], u[k + 2]);
    *reinterpret_cast<f32x2*>(redo + j * DH + 8 * c + 2 * r) = f32x2{t[0], t[1]};
    LDS_ORDER();
    if (lane == 0) LDS_ADD(lflag + 3, 1u << j);
    STAMP(6);
  }
  KA_WAIT();
  const __amdgpu_buffer_rsrc_t rs = __builtin_amdgcn_make_buffer_rsrc((void*)ws_, 0, WS_END, 0x00020000);
  if ((w >= 2 && w <= 9) || (DRY && w == 11)) {
    const bool dry = w == 11;
    const int tw = dry ? 8 : w - 2;
    const int ch = dry ? 0 : c < 10 ? c : 9;
    const GAS v4u* wop = (const GAS v4u*)(wo_ + (size_t)(h2 * DH + (dry ? 0 : 16 * tw + 4 * r)) * DM + c2 * CB + 8 * ch);
    lds_wait_ge(lflag + 2, dry ? 0 : nu);
    v4u wo[4];
#pragma unroll
    for (int jj = 0; jj < 4; ++jj) wo[jj] = ld_stream(wop + jj * (DM / 8));
    SB();
    const int sp = c & 7, pr = c >> 3;
    const int ooff = WS_O + (h2 * NSPLIT + sp) * (NREC * 1024) + (16 * tw + 4 * r + 2 * pr) * 8;
    unsigned p01[8], p23[8];
#pragma unroll
    for (int k = 0; k < 4; ++k) {
      p01[2 * k] = __builtin_amdgcn_perm(wo[1][k], wo[0][k], 0x05040100u);
      p01[2 * k + 1] = __builtin_amdgcn_perm(wo[1][k], wo[0][k], 0x07060302u);
      p23[2 * k] = __builtin_amdgcn_perm(wo[3][k], wo[2][k], 0x05040100u);
      p23[2 * k + 1] = __builtin_amdgcn_perm(wo[3][k], wo[2][k], 0x07060302u);
    }
    asm volatile("" : "+v"(p01[0]), "+v"(p01[1]), "+v"(p01[2]), "+v"(p01[3]), "+v"(p01[4]), "+v"(p01[5]), "+v"(p01[6]), "+v"(p01[7]));
    asm volatile("" : "+v"(p23[0]), "+v"(p23[1]), "+v"(p23[2]), "+v"(p23[3]), "+v"(p23[4]), "+v"(p23[5]), "+v"(p23[6]), "+v"(p23[7]));
    lds_wait_ge(lflag + 5, dry ? 0 : 1);
    unsigned bo0 = 0u, bo1 = 0u, bx = 0u;
    int go = dry ? OOB : ooff;
    for (unsigned spins = 0; spins <= SPIN_CAP; ++spins) {
      asm volatile("" : "+v"(go));
      const v4u ra = ld_sc1(rs, go);
      const bool to = (ra[1] ^ mix(ra[2], ra[3])) == tag;
      bo0 = to ? ra[0] : bo0; bo1 = to ? ra[2] : bo1; bx = to ? ra[3] : bx;
      go = to ? OOB : go;
      if (__builtin_amdgcn_ballot_w64(go != OOB) == 0ull) break;
      __builtin_amdgcn_s_sleep(SLP1);
    }
    if (!dry) STAMP(8);
    const unsigned bxo = dppu<XOR8>(bx);
    const float Ms = asf(pr ? bxo : bx), Ls = asf(pr ? bx : bxo);
    float Mh = Ms;
    Mh = fmaxf(Mh, dpp<XOR1>(Mh));
    Mh = fmaxf(Mh, dpp<XOR2>(Mh));
    Mh = fmaxf(Mh, dpp<XOR7>(Mh));
    const float ws = __builtin_amdgcn_exp2f((Ms - Mh) * LOG2E);
    float Lh = ws * Ls, o0 = ws * asf(bo0), o1 = ws * asf(bo1);
    Lh += dpp<XOR1>(Lh); o0 += dpp<XOR1>(o0); o1 += dpp<XOR1>(o1);
    Lh += dpp<XOR2>(Lh); o0 += dpp<XOR2>(o0); o1 += dpp<XOR2>(o1);
    Lh += dpp<XOR7>(Lh); o0 += dpp<XOR7>(o0); o1 += dpp<XOR7>(o1);
    const float inv = __builtin_amdgcn_rcpf(Lh);
    const unsigned mine = cvtpk(o0 * inv, o1 * inv);
    const unsigned other = dppu<XOR8>(mine);
    const unsigned o01 = pr ? other : mine, o23 = pr ? mine : other;
    float g8[8];
#pragma unroll
    for (int k = 0; k < 8; ++k) g8[k] = dot2(p23[k], o23, dot2(p01[k], o01, 0.f));
    float u[4], t[2];
#pragma unroll
    for (int k = 0; k < 4; ++k) u[k] = swap32_add(g8[k], g8[k + 4]);
#pragma unroll
    for (int k = 0; k < 2; ++k) t[k] = swap16_add(u[k], u[k + 2]);
    *reinterpret_cast<f32x2*>(redw + tw * DH + 8 * c + 2 * r) = f32x2{t[0], t[1]};
    LDS_ORDER();
    unsigned old = 0u;
    if (lane == 0) old = LDS_ADD(lflag + (dry ? 6 : 4), 1u);
    old = __builtin_amdgcn_readfirstlane(old);
    asm volatile("" ::: "memory");
    if (old == (dry ? 0u : 7u) && lane < CB / 2) {
      f32x2 o = {0.f, 0.f};
#pragma unroll
      for (int ww = 0; ww < 8; ++ww) o += *reinterpret_cast<const f32x2*>(redw + ww * DH + 2 * lane);
      const v4u rec3 = {asu(o[0]), tag, asu(o[1]), tag};
      const int cg = c2 * (CB / 16) + (lane >> 3);
      __builtin_amdgcn_raw_buffer_store_b128(rec3, rs, dry ? OOB : WS_H2 + cg * 4096 + h2 * 128 + (lane & 7) * 16, 0, AUX_SC1);
    }
    if (!dry) STAMP(9);
  } else if ((w == 10 || (DRY && w == 12)) && h2 < CB / 4) {
    const bool dry = w == 12;
    const int cg = c2 * (CB / 4) + h2;
    lds_wait_ge(lflag + GATE2W, dry ? 0 : 1);
    unsigned b0 = 0u, b1 = 0u;
    int go = dry ? OOB : WS_H2 + (cg >> 2) * 4096 + (lane & 31) * 128 + (cg & 3) * 32 + (lane >> 5) * 16;
    for (unsigned spins = 0; spins <= SPIN_CAP; ++spins) {
      asm volatile("" : "+v"(go));
      const v4u ra = ld_sc1(rs, go);
      const bool tv = tagged(ra, tag);
      b0 = tv ? ra[0] : b0; b1 = tv ? ra[2] : b1;
      go = tv ? OOB : go;
      if (__builtin_amdgcn_ballot_w64(go != OOB) == 0ull) break;
    }
    float a0 = asf(b0), a1 = asf(b1);
    a0 += dpp<XOR1>(a0); a1 += dpp<XOR1>(a1);
    a0 += dpp<XOR2>(a0); a1 += dpp<XOR2>(a1);
    a0 += dpp<XOR7>(a0); a1 += dpp<XOR7>(a1);
    a0 += dpp<XOR8>(a0); a1 += dpp<XOR8>(a1);
    a0 = swap16_add(a0, a0); a1 = swap16_add(a1, a1);
    const __amdgpu_buffer_rsrc_t ro = __builtin_amdgcn_make_buffer_rsrc((void*)out_, 0, DM * 2, 0x00020000);
    __builtin_amdgcn_raw_buffer_store_b32(cvtpk(a0, a1), ro, ((lane & 31) == 0 && !dry) ? 8 * cg + 4 * (lane >> 5) : OOB, 0, 0);
    if (dry) {
      const unsigned junk = __builtin_amdgcn_raw_buffer_load_b32(ro, lane == 0 ? 8 * cg : OOB, 0, 0);
      asm volatile("" ::"v"(junk));
    }
    if (cg == 0 && lane == 0 && !dry)
      __hip_atomic_store((GAS unsigned*)(ws_ + WS_EPOCH), tag, __ATOMIC_RELAXED, __HIP_MEMORY_SCOPE_AGENT);
    if (!dry) STAMP(10);
  }
  STAMP_OUT();
}

}

extern "C" void kernel_launch(void* const* d_in, const int* in_sizes, int n_in, void* d_out, int out_size, void* d_ws,
                              size_t ws_size, hipStream_t stream) {
  (void)in_sizes; (void)n_in; (void)out_size; (void)ws_size;
  decode_kernel<<<dim3(NH * NSPLIT), dim3(NTHR), 0, stream>>>(
      (const unsigned short*)d_in[0], (const unsigned short*)d_in[1], (const unsigned short*)d_in[2],
      (const unsigned short*)d_in[3], (const unsigned short*)d_in[4], (unsigned short*)d_out, (unsigned char*)d_ws);
}
```

```cpp
#include <hip/hip_runtime.h>
#include <hip/hip_bf16.h>
#include <stdint.h>

#define GAS __attribute__((address_space(1)))
#define LAS __attribute__((address_space(3)))
typedef unsigned v4u __attribute__((ext_vector_type(4)));
typedef unsigned v2u __attribute__((ext_vector_type(2)));
typedef unsigned v8u __attribute__((ext_vector_type(8)));
typedef float f32x2 __attribute__((ext_vector_type(2)));
typedef __bf16 bf16x2 __attribute__((ext_vector_type(2)));

namespace {

constexpr int DM = 640, NH = 32, DH = 128, SK = 16384, WIN = 4096;
constexpr int NSPLIT = 8;
constexpr int NKV = 15;
constexpr int NTHR = (NKV + 1) * 64;
constexpr int UT = 32;
constexpr int NU = WIN / UT / NSPLIT;
constexpr int QROWS_WG = DM / NSPLIT;
constexpr int QROWS_LANE = QROWS_WG / 4;
constexpr int CB = DM / NSPLIT;
constexpr int AHEAD = 2;
constexpr int WS_EPOCH = 0;
constexpr int WS_O = 32768;
constexpr int WS_Q = WS_O + NH * NSPLIT * 1024;
constexpr int WS_H2 = WS_Q + NH * NSPLIT * 1024;
constexpr int WS_END = WS_H2 + 40 * 4096;
constexpr int OOB = 0x7ffffff0;
constexpr unsigned SPIN_CAP = 1u << 18;
constexpr int AUX_SC1 = 16;

__device__ __forceinline__ float asf(unsigned u) { return __builtin_bit_cast(float, u); }
__device__ __forceinline__ unsigned asu(float f) { return __builtin_bit_cast(unsigned, f); }
__device__ __forceinline__ float dot2(unsigned a, unsigned b, float acc) {
  return __builtin_amdgcn_fdot2_f32_bf16(__builtin_bit_cast(bf16x2, a), __builtin_bit_cast(bf16x2, b), acc, false);
}
template <int CTRL> __device__ __forceinline__ float dpp(float x) {
  return asf((unsigned)__builtin_amdgcn_mov_dpp((int)asu(x), CTRL, 0xf, 0xf, true));
}
template <int CTRL> __device__ __forceinline__ unsigned dppu(unsigned x) {
  return (unsigned)__builtin_amdgcn_mov_dpp((int)x, CTRL, 0xf, 0xf, true);
}
constexpr int XOR1 = 0xB1, XOR2 = 0x4E, XOR7 = 0x141, XOR8 = 0x128;
__device__ __forceinline__ unsigned cvtpk(float lo, float hi) {
  f32x2 v = {lo, hi};
  return __builtin_bit_cast(unsigned, __builtin_convertvector(v, bf16x2));
}
__device__ __forceinline__ float bf16r(float x) { return asf(cvtpk(x, x) & 0xffff0000u); }
__device__ __forceinline__ float swap32_add(float a, float b) {
  auto r = __builtin_amdgcn_permlane32_swap(asu(a), asu(b), false, false);
  return asf(r[0]) + asf(r[1]);
}
__device__ __forceinline__ float swap16_add(float a, float b) {
  auto r = __builtin_amdgcn_permlane16_swap(asu(a), asu(b), false, false);
  return asf(r[0]) + asf(r[1]);
}
__device__ __forceinline__ float swap32_max(float a) {
  auto r = __builtin_amdgcn_permlane32_swap(asu(a), asu(a), false, false);
  return fmaxf(asf(r[0]), asf(r[1]));
}
__device__ __forceinline__ float swap16_max(float a) {
  auto r = __builtin_amdgcn_permlane16_swap(asu(a), asu(a), false, false);
  return fmaxf(asf(r[0]), asf(r[1]));
}
__device__ __forceinline__ v4u ld_stream(const GAS v4u* p) { return __builtin_nontemporal_load(p); }
#define SB() __builtin_amdgcn_sched_barrier(0)
#define LDS_ST(p, v) __hip_atomic_store((p), (v), __ATOMIC_RELAXED, __HIP_MEMORY_SCOPE_WORKGROUP)
#define LDS_LD(p) __hip_atomic_load((p), __ATOMIC_RELAXED, __HIP_MEMORY_SCOPE_WORKGROUP)
#define LDS_ADD(p, v) __hip_atomic_fetch_add((p), (v), __ATOMIC_RELAXED, __HIP_MEMORY_SCOPE_WORKGROUP)
#define LDS_ORDER() asm volatile("" ::: "memory")
__device__ __forceinline__ void wg_barrier() {
  SB();
  asm volatile("s_waitcnt lgkmcnt(0)" ::: "memory");
  __builtin_amdgcn_s_barrier();
  asm volatile("" ::: "memory");
  SB();
}
__device__ __forceinline__ void lds_wait_ge(LAS unsigned* p, int want) {
#pragma nounroll
  for (unsigned spins = 0; spins < (SPIN_CAP << 4); ++spins) {
    if ((int)__builtin_amdgcn_readfirstlane(LDS_LD(p)) >= want) break;
    __builtin_amdgcn_s_sleep(1);
  }
  asm volatile("" ::: "memory");
}
__device__ __forceinline__ v4u ld_sc1(__amdgpu_buffer_rsrc_t rs, int off) {
  return __builtin_amdgcn_raw_buffer_load_b128(rs, off, 0, AUX_SC1);
}
__device__ __forceinline__ unsigned mix(unsigned b, unsigned x) {
  return __builtin_amdgcn_alignbit(b, b, 7) ^ __builtin_amdgcn_alignbit(x, x, 19);
}
__device__ __forceinline__ bool tagged(const v4u& r, unsigned tag) { return (r[1] == tag) & (r[3] == tag); }

__global__ void __launch_bounds__(NTHR, 4)
decode_kernel(const unsigned short* __restrict__, const unsigned short* __restrict__, const unsigned short* __restrict__,
              const unsigned short* __restrict__, const unsigned short* __restrict__, unsigned short* __restrict__,
              unsigned char*) {
  v8u ka0;
  v4u ka1;
  v2u ka2;
  asm volatile("s_load_dwordx8 %0, %3, 0x0\n\ts_load_dwordx4 %1, %3, 0x20\n\ts_load_dwordx2 %2, %3, 0x30"
               : "=&s"(ka0), "=&s"(ka1), "=&s"(ka2)
               : "s"(__builtin_amdgcn_kernarg_segment_ptr()));
#define KA_WAIT() asm volatile("s_waitcnt lgkmcnt(0)" : "+s"(ka0), "+s"(ka1), "+s"(ka2))
#define KA_PTR(T, lo, hi) ((T)(((unsigned long long)(hi) << 32) | (lo)))
#define x_ KA_PTR(const unsigned short*, ka0[0], ka0[1])
#define wq_ KA_PTR(const unsigned short*, ka0[6], ka0[7])
#define wo_ KA_PTR(const unsigned short*, ka1[0], ka1[1])
#define out_ KA_PTR(unsigned short*, ka1[2], ka1[3])
#define ws_ KA_PTR(unsigned char*, ka2[0], ka2[1])
  __shared__ __attribute__((aligned(16))) float lds[17 * DH + 9 * DH + 64 + 64 + 16];
  float* redo = lds;
  float* redw = lds + 17 * DH;
  unsigned* qs = reinterpret_cast<unsigned*>(lds + 26 * DH);
  float* wml = lds + 26 * DH + 64;
  LAS unsigned* lflag = (LAS unsigned*)(wml + 40);
  const int tid = threadIdx.x, lane = tid & 63;
  const int wv = __builtin_amdgcn_readfirstlane(tid >> 6);
  const int w = wv - 1;
  const int r = lane >> 4, c = lane & 15;
  const int b = blockIdx.x;
  const int h = b >> 3, s = b & 7;
  const int h2 = (b & 7) * 4 + ((b >> 3) & 3), c2 = b >> 5;
  constexpr float LOG2E = 1.4426950408889634f;
  const int nu = NU + ((h & 1) ? ((s & 1) ? -1 : 1) : 0);

  if (__builtin_expect(wv == 0, 1)) {
    __builtin_amdgcn_s_setprio(3);
    KA_WAIT();
    const __amdgpu_buffer_rsrc_t rs = __builtin_amdgcn_make_buffer_rsrc((void*)ws_, 0, WS_END, 0x00020000);
    const unsigned ep = __hip_atomic_load((GAS unsigned*)(ws_ + WS_EPOCH), __ATOMIC_RELAXED, __HIP_MEMORY_SCOPE_AGENT);
    const GAS v2u* xp = (const GAS v2u*)(x_ + QROWS_WG * s + QROWS_LANE * r);
    v2u xv[5];
#pragma unroll
    for (int i = 0; i < 5; ++i) xv[i] = xp[i];
    SB();
    const GAS v4u* wqp = (const GAS v4u*)(wq_ + (size_t)(QROWS_WG * s + QROWS_LANE * r) * (NH * DH) + h * DH + 8 * c);
    v4u wq[QROWS_LANE];
#pragma unroll
    for (int i = 0; i < QROWS_LANE; ++i) wq[i] = ld_stream(wqp + (size_t)i * (NH * DH / 8));
    SB();
    if (lane < 16) LDS_ST(lflag + lane, 0u);
    wg_barrier();
    float qa[8];
#pragma unroll
    for (int k = 0; k < 8; ++k) qa[k] = 0.f;
#pragma unroll
    for (int j = 0; j < QROWS_LANE / 2; ++j) {
      const unsigned xpair = xv[j >> 1][j & 1];
#pragma unroll
      for (int k = 0; k < 4; ++k) {
        const unsigned a = wq[2 * j][k], bb = wq[2 * j + 1][k];
        qa[2 * k] = dot2(__builtin_amdgcn_perm(bb, a, 0x05040100u), xpair, qa[2 * k]);
        qa[2 * k + 1] = dot2(__builtin_amdgcn_perm(bb, a, 0x07060302u), xpair, qa[2 * k + 1]);
      }
    }
    const unsigned tg = ep + 1u + (ep == 0xffffffffu);
    float u[4], t[2];
#pragma unroll
    for (int k = 0; k < 4; ++k) u[k] = swap32_add(qa[k], qa[k + 4]);
#pragma unroll
    for (int k = 0; k < 2; ++k) t[k] = swap16_add(u[k], u[k + 2]);
    const v4u rec = {asu(t[0]), tg, asu(t[1]), tg};
    __builtin_amdgcn_raw_buffer_store_b128(rec, rs, WS_Q + (h * NSPLIT + s) * 1024 + (8 * c + 2 * r) * 8, 0, AUX_SC1);
    v4u g[NSPLIT];
#pragma unroll
    for (int j = 0; j < NSPLIT; ++j) g[j] = ld_sc1(rs, WS_Q + (h * NSPLIT + j) * 1024 + lane * 16);
    for (unsigned spins = 0;; ++spins) {
      bool ok = true;
#pragma unroll
      for (int j = 0; j < NSPLIT; ++j) ok &= tagged(g[j], tg);
      if (__all(ok) || spins > SPIN_CAP) break;
      __builtin_amdgcn_s_sleep(1);
#pragma unroll
      for (int j = 0; j < NSPLIT; ++j)
        if (!tagged(g[j], tg)) g[j] = ld_sc1(rs, WS_Q + (h * NSPLIT + j) * 1024 + lane * 16);
    }
    float q0 = 0.f, q1 = 0.f;
#pragma unroll
    for (int j = 0; j < NSPLIT; ++j) { q0 += asf(g[j][0]); q1 += asf(g[j][2]); }
    qs[lane] = cvtpk(q0, q1);
    if (lane == 0) LDS_ST(lflag + 1, tg);
    LDS_ORDER();
    if (lane == 0) LDS_ST(lflag, 1u);
    float M = -INFINITY, L = 0.f, o0 = 0.f, o1 = 0.f;
#pragma unroll 1
    for (int j = 0; j < nu; ++j) {
      for (unsigned spins = 0; spins < (SPIN_CAP << 4); ++spins)
        if ((__builtin_amdgcn_readfirstlane(LDS_LD(lflag + 3)) >> j) & 1u) break;
      asm volatile("" ::: "memory");
      const f32x2 ml = *reinterpret_cast<const f32x2*>(wml + 2 * j);
      const f32x2 ov = *reinterpret_cast<const f32x2*>(redo + j * DH + 2 * lane);
      const float Mn = fmaxf(M, ml[0]);
      const float a = __builtin_amdgcn_exp2f((M - Mn) * LOG2E), f = __builtin_amdgcn_exp2f((ml[0] - Mn) * LOG2E);
      o0 = fmaf(o0, a, f * ov[0]);
      o1 = fmaf(o1, a, f * ov[1]);
      L = fmaf(L, a, f * ml[1]);
      M = Mn;
      asm volatile("" : "+v"(o0), "+v"(o1), "+v"(L), "+v"(M));
    }
    const unsigned xw = asu((lane & 1) ? L : M);
    const v4u rec1 = {asu(o0), tg ^ mix(asu(o1), xw), asu(o1), xw};
    __builtin_amdgcn_raw_buffer_store_b128(rec1, rs, WS_O + (h * NSPLIT + s) * 1024 + lane * 16, 0, AUX_SC1);
    if (lane == 0) LDS_ST(lflag + 5, 1u);
    return;
  }

  const size_t kvbase = ((size_t)h * SK + (SK - WIN)) * DH * 2;
  const unsigned loff = lane * 16;
  const int rot = h >> 2;
  v4u qp = {0u, 0u, 0u, 0u};
  unsigned tag = 0u;
  if (w >= AHEAD) wg_barrier();
  for (int j = w; j < nu; j += NKV) {
    const int jj = j < NU ? j : NU - 1, ss = j < NU ? s : s + 1;
    const int unit = ((jj + h) & (NU - 1)) * NSPLIT + ((ss + jj) & 7);
    if (j >= AHEAD) lds_wait_ge(lflag + 2, j - AHEAD + 1);
    size_t uoff = kvbase + (size_t)unit * (UT * DH * 2);
    asm volatile("" : "+s"(uoff));
    unsigned long long kb, vb;
    asm volatile("s_waitcnt lgkmcnt(0)\n\ts_mov_b64 %0, %2\n\ts_mov_b64 %1, %3"
                 : "=&s"(kb), "=&s"(vb)
                 : "s"(__builtin_shufflevector(ka0, ka0, 2, 3)), "s"(__builtin_shufflevector(ka0, ka0, 4, 5)));
    const GAS unsigned char* kp = (const GAS unsigned char*)kb + uoff;
    const GAS unsigned char* vp = (const GAS unsigned char*)vb + uoff;
    v4u kk[8], vv[8];
    SB();
#pragma unroll
    for (int i = 0; i < 8; ++i) kk[i] = ld_stream((const GAS v4u*)(kp + (((i + rot) & 7) << 10) + loff));
#pragma unroll
    for (int i = 0; i < 8; ++i) vv[i] = ld_stream((const GAS v4u*)(vp + (((i + rot) & 7) << 10) + loff));
    SB();
    if (j < AHEAD) wg_barrier();
    asm volatile("s_waitcnt vmcnt(15)" ::: "memory");
    if (lane == 0) LDS_ADD(lflag + 2, 1u);
    SB();
    if (j == w) {
      lds_wait_ge(lflag, 1);
      qp = *reinterpret_cast<const v4u*>(qs + 4 * c);
      tag = LDS_LD(lflag + 1);
    }
    float sc[8];
#pragma unroll
    for (int i = 0; i < 8; ++i) {
      float d = 0.f;
#pragma unroll
      for (int k = 0; k < 4; ++k) d = dot2(kk[i][k], qp[k], d);
      sc[i] = d;
    }
#define TR_STEP(HALF, CTRL, BIT)                                  \
  _Pragma("unroll") for (int i = 0; i < (HALF); ++i) {            \
    const float lo_ = sc[i], hi_ = sc[i + (HALF)];                \
    const bool up_ = (c & (BIT)) != 0;                            \
    const float keep_ = up_ ? hi_ : lo_, send_ = up_ ? lo_ : hi_; \
    sc[i] = keep_ + dpp<CTRL>(send_);                             \
  }
    TR_STEP(4, XOR8, 8) TR_STEP(2, XOR7, 4) TR_STEP(1, XOR2, 2)
#undef TR_STEP
    const float sraw = sc[0] + dpp<XOR1>(sc[0]);
    const float sv = bf16r(bf16r(sraw) * 0.08838834764831845f);
    float mx = sv;
    mx = fmaxf(mx, dpp<XOR2>(mx));
    mx = fmaxf(mx, dpp<XOR7>(mx));
    mx = fmaxf(mx, dpp<XOR8>(mx));
    mx = swap16_max(mx);
    mx = swap32_max(mx);
    const float p = __builtin_amdgcn_exp2f((sv - mx) * LOG2E);
    float l = p;
    l += dpp<XOR2>(l);
    l += dpp<XOR7>(l);
    l += dpp<XOR8>(l);
    l = swap16_add(l, l);
    l = swap32_add(l, l);
    if (lane == 0) *reinterpret_cast<f32x2*>(wml + 2 * j) = f32x2{mx, l};
    float acc[8];
#pragma unroll
    for (int k = 0; k < 8; ++k) acc[k] = 0.f;
#define PV_STEP(I)                                                       \
  {                                                                      \
    const float pi_ = dpp<0x150 + 2 * (I)>(p);          \
    _Pragma("unroll") for (int k = 0; k < 4; ++k) {                      \
      const unsigned u_ = vv[I][k];                                      \
      acc[2 * k] = fmaf(pi_, asf(u_ << 16), acc[2 * k]);                 \
      acc[2 * k + 1] = fmaf(pi_, asf(u_ & 0xffff0000u), acc[2 * k + 1]); \
    }                                                                    \
    asm volatile("" : "+v"(acc[0]), "+v"(acc[1]), "+v"(acc[2]), "+v"(acc[3]), "+v"(acc[4]), "+v"(acc[5]), "+v"(acc[6]), "+v"(acc[7])); \
  }
    PV_STEP(0) PV_STEP(1) PV_STEP(2) PV_STEP(3) PV_STEP(4) PV_STEP(5) PV_STEP(6) PV_STEP(7)
#undef PV_STEP
    float u[4], t[2];
#pragma unroll
    for (int k = 0; k < 4; ++k) u[k] = swap32_add(acc[k], acc[k + 4]);
#pragma unroll
    for (int k = 0; k < 2; ++k) t[k] = swap16_add(u[k], u[k + 2]);
    *reinterpret_cast<f32x2*>(redo + j * DH + 8 * c + 2 * r) = f32x2{t[0], t[1]};
    LDS_ORDER();
    if (lane == 0) LDS_ADD(lflag + 3, 1u << j);
  }
  KA_WAIT();
  const __amdgpu_buffer_rsrc_t rs = __builtin_amdgcn_make_buffer_rsrc((void*)ws_, 0, WS_END, 0x00020000);
  if ((w >= 2 && w <= 9) || w == 11) {
    const bool dry = w == 11;
    const int tw = dry ? 8 : w - 2;
    const int ch = dry ? 0 : c < 10 ? c : 9;
    const GAS v4u* wop = (const GAS v4u*)(wo_ + (size_t)(h2 * DH + (dry ? 0 : 16 * tw + 4 * r)) * DM + c2 * CB + 8 * ch);
    lds_wait_ge(lflag + 2, dry ? 0 : nu);
    v4u wo[4];
#pragma unroll
    for (int jj = 0; jj < 4; ++jj) wo[jj] = ld_stream(wop + jj * (DM / 8));
    SB();
    const int sp = c & 7, pr = c >> 3;
    const int ooff = WS_O + (h2 * NSPLIT + sp) * 1024 + (16 * tw + 4 * r + 2 * pr) * 8;
    unsigned p01[8], p23[8];
#pragma unroll
    for (int k = 0; k < 4; ++k) {
      p01[2 * k] = __builtin_amdgcn_perm(wo[1][k], wo[0][k], 0x05040100u);
      p01[2 * k + 1] = __builtin_amdgcn_perm(wo[1][k], wo[0][k], 0x07060302u);
      p23[2 * k] = __builtin_amdgcn_perm(wo[3][k], wo[2][k], 0x05040100u);
      p23[2 * k + 1] = __builtin_amdgcn_perm(wo[3][k], wo[2][k], 0x07060302u);
    }
    asm volatile("" : "+v"(p01[0]), "+v"(p01[1]), "+v"(p01[2]), "+v"(p01[3]), "+v"(p01[4]), "+v"(p01[5]), "+v"(p01[6]), "+v"(p01[7]));
    asm volatile("" : "+v"(p23[0]), "+v"(p23[1]), "+v"(p23[2]), "+v"(p23[3]), "+v"(p23[4]), "+v"(p23[5]), "+v"(p23[6]), "+v"(p23[7]));
    lds_wait_ge(lflag + 5, dry ? 0 : 1);
    unsigned bo0 = 0u, bo1 = 0u, bx = 0u;
    int go = dry ? OOB : ooff;
    for (unsigned spins = 0; spins <= SPIN_CAP; ++spins) {
      asm volatile("" : "+v"(go));
      const v4u ra = ld_sc1(rs, go);
      const bool to = (ra[1] ^ mix(ra[2], ra[3])) == tag;
      bo0 = to ? ra[0] : bo0; bo1 = to ? ra[2] : bo1; bx = to ? ra[3] : bx;
      go = to ? OOB : go;
      if (__builtin_amdgcn_ballot_w64(go != OOB) == 0ull) break;
      __builtin_amdgcn_s_sleep(1);
    }
    const unsigned bxo = dppu<XOR8>(bx);
    const float Ms = asf(pr ? bxo : bx), Ls = asf(pr ? bx : bxo);
    float Mh = Ms;
    Mh = fmaxf(Mh, dpp<XOR1>(Mh));
    Mh = fmaxf(Mh, dpp<XOR2>(Mh));
    Mh = fmaxf(Mh, dpp<XOR7>(Mh));
    const float ws = __builtin_amdgcn_exp2f((Ms - Mh) * LOG2E);
    float Lh = ws * Ls, o0 = ws * asf(bo0), o1 = ws * asf(bo1);
    Lh += dpp<XOR1>(Lh); o0 += dpp<XOR1>(o0); o1 += dpp<XOR1>(o1);
    Lh += dpp<XOR2>(Lh); o0 += dpp<XOR2>(o0); o1 += dpp<XOR2>(o1);
    Lh += dpp<XOR7>(Lh); o0 += dpp<XOR7>(o0); o1 += dpp<XOR7>(o1);
    const float inv = __builtin_amdgcn_rcpf(Lh);
    const unsigned mine = cvtpk(o0 * inv, o1 * inv);
    const unsigned other = dppu<XOR8>(mine);
    const unsigned o01 = pr ? other : mine, o23 = pr ? mine : other;
    float g8[8];
#pragma unroll
    for (int k = 0; k < 8; ++k) g8[k] = dot2(p23[k], o23, dot2(p01[k], o01, 0.f));
    float u[4], t[2];
#pragma unroll
    for (int k = 0; k < 4; ++k) u[k] = swap32_add(g8[k], g8[k + 4]);
#pragma unroll
    for (int k = 0; k < 2; ++k) t[k] = swap16_add(u[k], u[k + 2]);
    *reinterpret_cast<f32x2*>(redw + tw * DH + 8 * c + 2 * r) = f32x2{t[0], t[1]};
    LDS_ORDER();
    unsigned old = 0u;
    if (lane == 0) old = LDS_ADD(lflag + (dry ? 6 : 4), 1u);
    old = __builtin_amdgcn_readfirstlane(old);
    asm volatile("" ::: "memory");
    if (old == (dry ? 0u : 7u) && lane < CB / 2) {
      f32x2 o = {0.f, 0.f};
#pragma unroll
      for (int ww = 0; ww < 8; ++ww) o += *reinterpret_cast<const f32x2*>(redw + ww * DH + 2 * lane);
      const v4u rec3 = {asu(o[0]), tag, asu(o[1]), tag};
      const int cg = c2 * (CB / 16) + (lane >> 3);
      __builtin_amdgcn_raw_buffer_store_b128(rec3, rs, dry ? OOB : WS_H2 + cg * 4096 + h2 * 128 + (lane & 7) * 16, 0, AUX_SC1);
    }
  } else if ((w == 10 || w == 12) && h2 < CB / 4) {
    const bool dry = w == 12;
    const int cg = c2 * (CB / 4) + h2;
    lds_wait_ge(lflag + 4, dry ? 0 : 1);
    unsigned b0 = 0u, b1 = 0u;
    int go = dry ? OOB : WS_H2 + (cg >> 2) * 4096 + (lane & 31) * 128 + (cg & 3) * 32 + (lane >> 5) * 16;
    for (unsigned spins = 0; spins <= SPIN_CAP; ++spins) {
      asm volatile("" : "+v"(go));
      const v4u ra = ld_sc1(rs, go);
      const bool tv = tagged(ra, tag);
      b0 = tv ? ra[0] : b0; b1 = tv ? ra[2] : b1;
      go = tv ? OOB : go;
      if (__builtin_amdgcn_ballot_w64(go != OOB) == 0ull) break;
    }
    float a0 = asf(b0), a1 = asf(b1);
    a0 += dpp<XOR1>(a0); a1 += dpp<XOR1>(a1);
    a0 += dpp<XOR2>(a0); a1 += dpp<XOR2>(a1);
    a0 += dpp<XOR7>(a0); a1 += dpp<XOR7>(a1);
    a0 += dpp<XOR8>(a0); a1 += dpp<XOR8>(a1);
    a0 = swap16_add(a0, a0); a1 = swap16_add(a1, a1);
    const __amdgpu_buffer_rsrc_t ro = __builtin_amdgcn_make_buffer_rsrc((void*)out_, 0, DM * 2, 0x00020000);
    __builtin_amdgcn_raw_buffer_store_b32(cvtpk(a0, a1), ro, ((lane & 31) == 0 && !dry) ? 8 * cg + 4 * (lane >> 5) : OOB, 0, 0);
    if (dry) {
      const unsigned junk = __builtin_amdgcn_raw_buffer_load_b32(ro, lane == 0 ? 8 * cg : OOB, 0, 0);
      asm volatile("" ::"v"(junk));
    }
    if (cg == 0 && lane == 0 && !dry)
      __hip_atomic_store((GAS unsigned*)(ws_ + WS_EPOCH), tag, __ATOMIC_RELAXED, __HIP_MEMORY_SCOPE_AGENT);
  }
}

}

extern "C" void kernel_launch(void* const* d_in, const int* in_sizes, int n_in, void* d_out, int out_size, void* d_ws,
                              size_t ws_size, hipStream_t stream) {
  (void)in_sizes; (void)n_in; (void)out_size; (void)ws_size;
  decode_kernel<<<dim3(NH * NSPLIT), dim3(NTHR), 0, stream>>>(
      (const unsigned short*)d_in[0], (const unsigned short*)d_in[1], (const unsigned short*)d_in[2],
      (const unsigned short*)d_in[3], (const unsigned short*)d_in[4], (unsigned short*)d_out, (unsigned char*)d_ws);
}
```

```cpp
#include <hip/hip_runtime.h>
#include <hip/hip_bf16.h>
#include <stdint.h>

#define GAS __attribute__((address_space(1)))
#define LAS __attribute__((address_space(3)))
typedef unsigned v4u __attribute__((ext_vector_type(4)));
typedef unsigned v2u __attribute__((ext_vector_type(2)));
typedef unsigned v8u __attribute__((ext_vector_type(8)));
typedef float f32x2 __attribute__((ext_vector_type(2)));
typedef __bf16 bf16x2 __attribute__((ext_vector_type(2)));

namespace {

constexpr int DM = 640, NH = 32, DH = 128, SK = 16384, WIN = 4096;
constexpr int NSPLIT = 8;
constexpr int NKV = 15;
constexpr int NTHR = (NKV + 1) * 64;
constexpr int UT = 32;
constexpr int NU = WIN / UT / NSPLIT;
constexpr int QROWS_WG = DM / NSPLIT;
constexpr int QROWS_LANE = QROWS_WG / 4;
constexpr int CB = DM / NSPLIT;
constexpr int AHEAD = 3;
constexpr int WS_EPOCH = 0;
constexpr int WS_O = 32768;
constexpr int WS_Q = WS_O + NH * NSPLIT * 1024;
constexpr int WS_H2 = WS_Q + NH * NSPLIT * 1024;
constexpr int WS_END = WS_H2 + 40 * 4096;
constexpr int OOB = 0x7ffffff0;
constexpr unsigned SPIN_CAP = 1u << 18;
constexpr int AUX_SC1 = 16;

__device__ __forceinline__ float asf(unsigned u) { return __builtin_bit_cast(float, u); }
__device__ __forceinline__ unsigned asu(float f) { return __builtin_bit_cast(unsigned, f); }
__device__ __forceinline__ float dot2(unsigned a, unsigned b, float acc) {
  return __builtin_amdgcn_fdot2_f32_bf16(__builtin_bit_cast(bf16x2, a), __builtin_bit_cast(bf16x2, b), acc, false);
}
template <int CTRL> __device__ __forceinline__ float dpp(float x) {
  return asf((unsigned)__builtin_amdgcn_mov_dpp((int)asu(x), CTRL, 0xf, 0xf, true));
}
template <int CTRL> __device__ __forceinline__ unsigned dppu(unsigned x) {
  return (unsigned)__builtin_amdgcn_mov_dpp((int)x, CTRL, 0xf, 0xf, true);
}
constexpr int XOR1 = 0xB1, XOR2 = 0x4E, XOR7 = 0x141, XOR8 = 0x128;
__device__ __forceinline__ unsigned cvtpk(float lo, float hi) {
  f32x2 v = {lo, hi};
  return __builtin_bit_cast(unsigned, __builtin_convertvector(v, bf16x2));
}
__device__ __forceinline__ float bf16r(float x) { return asf(cvtpk(x, x) & 0xffff0000u); }
__device__ __forceinline__ float swap32_add(float a, float b) {
  auto r = __builtin_amdgcn_permlane32_swap(asu(a), asu(b), false, false);
  return asf(r[0]) + asf(r[1]);
}
__device__ __forceinline__ float swap16_add(float a, float b) {
  auto r = __builtin_amdgcn_permlane16_swap(asu(a), asu(b), false, false);
  return asf(r[0]) + asf(r[1]);
}
__device__ __forceinline__ float swap32_max(float a) {
  auto r = __builtin_amdgcn_permlane32_swap(asu(a), asu(a), false, false);
  return fmaxf(asf(r[0]), asf(r[1]));
}
__device__ __forceinline__ float swap16_max(float a) {
  auto r = __builtin_amdgcn_permlane16_swap(asu(a), asu(a), false, false);
  return fmaxf(asf(r[0]), asf(r[1]));
}
__device__ __forceinline__ v4u ld_stream(const GAS v4u* p) { return __builtin_nontemporal_load(p); }
#define SB() __builtin_amdgcn_sched_barrier(0)
#define LDS_ST(p, v) __hip_atomic_store((p), (v), __ATOMIC_RELAXED, __HIP_MEMORY_SCOPE_WORKGROUP)
#define LDS_LD(p) __hip_atomic_load((p), __ATOMIC_RELAXED, __HIP_MEMORY_SCOPE_WORKGROUP)
#define LDS_ADD(p, v) __hip_atomic_fetch_add((p), (v), __ATOMIC_RELAXED, __HIP_MEMORY_SCOPE_WORKGROUP)
#define LDS_ORDER() asm volatile("" ::: "memory")
__device__ __forceinline__ void wg_barrier() {
  SB();
  asm volatile("s_waitcnt lgkmcnt(0)" ::: "memory");
  __builtin_amdgcn_s_barrier();
  asm volatile("" ::: "memory");
  SB();
}
__device__ __forceinline__ void lds_wait_ge(LAS unsigned* p, int want) {
#pragma nounroll
  for (unsigned spins = 0; spins < (SPIN_CAP << 4); ++spins) {
    if ((int)__builtin_amdgcn_readfirstlane(LDS_LD(p)) >= want) break;
    __builtin_amdgcn_s_sleep(1);
  }
  asm volatile("" ::: "memory");
}
__device__ __forceinline__ v4u ld_sc1(__amdgpu_buffer_rsrc_t rs, int off) {
  return __builtin_amdgcn_raw_buffer_load_b128(rs, off, 0, AUX_SC1);
}
__device__ __forceinline__ unsigned mix(unsigned b, unsigned x) {
  return __builtin_amdgcn_alignbit(b, b, 7) ^ __builtin_amdgcn_alignbit(x, x, 19);
}
__device__ __forceinline__ bool tagged(const v4u& r, unsigned tag) { return (r[1] == tag) & (r[3] == tag); }

__global__ void __launch_bounds__(NTHR, 4)
decode_kernel(const unsigned short* __restrict__, const unsigned short* __restrict__, const unsigned short* __restrict__,
              const unsigned short* __restrict__, const unsigned short* __restrict__, unsigned short* __restrict__,
              unsigned char*) {
  v8u ka0;
  v4u ka1;
  v2u ka2;
  asm volatile("s_load_dwordx8 %0, %3, 0x0\n\ts_load_dwordx4 %1, %3, 0x20\n\ts_load_dwordx2 %2, %3, 0x30"
               : "=&s"(ka0), "=&s"(ka1), "=&s"(ka2)
               : "s"(__builtin_amdgcn_kernarg_segment_ptr()));
#define KA_WAIT() asm volatile("s_waitcnt lgkmcnt(0)" : "+s"(ka0), "+s"(ka1), "+s"(ka2))
#define KA_PTR(T, lo, hi) ((T)(((unsigned long long)(hi) << 32) | (lo)))
#define x_ KA_PTR(const unsigned short*, ka0[0], ka0[1])
#define wq_ KA_PTR(const unsigned short*, ka0[6], ka0[7])
#define wo_ KA_PTR(const unsigned short*, ka1[0], ka1[1])
#define out_ KA_PTR(unsigned short*, ka1[2], ka1[3])
#define ws_ KA_PTR(unsigned char*, ka2[0], ka2[1])
  __shared__ __attribute__((aligned(16))) float lds[17 * DH + 9 * DH + 64 + 64 + 16];
  float* redo = lds;
  float* redw = lds + 17 * DH;
  unsigned* qs = reinterpret_cast<unsigned*>(lds + 26 * DH);
  float* wml = lds + 26 * DH + 64;
  LAS unsigned* lflag = (LAS unsigned*)(wml + 40);
  const int tid = threadIdx.x, lane = tid & 63;
  const int wv = __builtin_amdgcn_readfirstlane(tid >> 6);
  const int w = wv - 1;
  const int r = lane >> 4, c = lane & 15;
  const int b = blockIdx.x;
  const int h = b >> 3, s = b & 7;
  const int h2 = (b & 7) * 4 + ((b >> 3) & 3), c2 = b >> 5;
  constexpr float LOG2E = 1.4426950408889634f;
  const int nu = NU + ((h & 1) ? ((s & 1) ? -1 : 1) : 0);

  if (__builtin_expect(wv == 0, 1)) {
    __builtin_amdgcn_s_setprio(3);
    KA_WAIT();
    const __amdgpu_buffer_rsrc_t rs = __builtin_amdgcn_make_buffer_rsrc((void*)ws_, 0, WS_END, 0x00020000);
    const unsigned ep = __hip_atomic_load((GAS unsigned*)(ws_ + WS_EPOCH), __ATOMIC_RELAXED, __HIP_MEMORY_SCOPE_AGENT);
    const GAS v2u* xp = (const GAS v2u*)(x_ + QROWS_WG * s + QROWS_LANE * r);
    v2u xv[5];
#pragma unroll
    for (int i = 0; i < 5; ++i) xv[i] = xp[i];
    SB();
    const GAS v4u* wqp = (const GAS v4u*)(wq_ + (size_t)(QROWS_WG * s + QROWS_LANE * r) * (NH * DH) + h * DH + 8 * c);
    v4u wq[QROWS_LANE];
#pragma unroll
    for (int i = 0; i < QROWS_LANE; ++i) wq[i] = ld_stream(wqp + (size_t)i * (NH * DH / 8));
    SB();
    if (lane < 16) LDS_ST(lflag + lane, 0u);
    wg_barrier();
    float qa[8];
#pragma unroll
    for (int k = 0; k < 8; ++k) qa[k] = 0.f;
#pragma unroll
    for (int j = 0; j < QROWS_LANE / 2; ++j) {
      const unsigned xpair = xv[j >> 1][j & 1];
#pragma unroll
      for (int k = 0; k < 4; ++k) {
        const unsigned a = wq[2 * j][k], bb = wq[2 * j + 1][k];
        qa[2 * k] = dot2(__builtin_amdgcn_perm(bb, a, 0x05040100u), xpair, qa[2 * k]);
        qa[2 * k + 1] = dot2(__builtin_amdgcn_perm(bb, a, 0x07060302u), xpair, qa[2 * k + 1]);
      }
    }
    const unsigned tg = ep + 1u + (ep == 0xffffffffu);
    float u[4], t[2];
#pragma unroll
    for (int k = 0; k < 4; ++k) u[k] = swap32_add(qa[k], qa[k + 4]);
#pragma unroll
    for (int k = 0; k < 2; ++k) t[k] = swap16_add(u[k], u[k + 2]);
    const v4u rec = {asu(t[0]), tg, asu(t[1]), tg};
    __builtin_amdgcn_raw_buffer_store_b128(rec, rs, WS_Q + (h * NSPLIT + s) * 1024 + (8 * c + 2 * r) * 8, 0, AUX_SC1);
    v4u g[NSPLIT];
#pragma unroll
    for (int j = 0; j < NSPLIT; ++j) g[j] = ld_sc1(rs, WS_Q + (h * NSPLIT + j) * 1024 + lane * 16);
    for (unsigned spins = 0;; ++spins) {
      bool ok = true;
#pragma unroll
      for (int j = 0; j < NSPLIT; ++j) ok &= tagged(g[j], tg);
      if (__all(ok) || spins > SPIN_CAP) break;
      __builtin_amdgcn_s_sleep(1);
#pragma unroll
      for (int j = 0; j < NSPLIT; ++j)
        if (!tagged(g[j], tg)) g[j] = ld_sc1(rs, WS_Q + (h * NSPLIT + j) * 1024 + lane * 16);
    }
    float q0 = 0.f, q1 = 0.f;
#pragma unroll
    for (int j = 0; j < NSPLIT; ++j) { q0 += asf(g[j][0]); q1 += asf(g[j][2]); }
    qs[lane] = cvtpk(q0, q1);
    if (lane == 0) LDS_ST(lflag + 1, tg);
    LDS_ORDER();
    if (lane == 0) LDS_ST(lflag, 1u);
    float M = -INFINITY, L = 0.f, o0 = 0.f, o1 = 0.f;
#pragma unroll 1
    for (int j = 0; j < nu; ++j) {
      for (unsigned spins = 0; spins < (SPIN_CAP << 4); ++spins)
        if ((__builtin_amdgcn_readfirstlane(LDS_LD(lflag + 3)) >> j) & 1u) break;
      asm volatile("" ::: "memory");
      const f32x2 ml = *reinterpret_cast<const f32x2*>(wml + 2 * j);
      const f32x2 ov = *reinterpret_cast<const f32x2*>(redo + j * DH + 2 * lane);
      const float Mn = fmaxf(M, ml[0]);
      const float a = __builtin_amdgcn_exp2f((M - Mn) * LOG2E), f = __builtin_amdgcn_exp2f((ml[0] - Mn) * LOG2E);
      o0 = fmaf(o0, a, f * ov[0]);
      o1 = fmaf(o1, a, f * ov[1]);
      L = fmaf(L, a, f * ml[1]);
      M = Mn;
      asm volatile("" : "+v"(o0), "+v"(o1), "+v"(L), "+v"(M));
    }
    const unsigned xw = asu((lane & 1) ? L : M);
    const v4u rec1 = {asu(o0), tg ^ mix(asu(o1), xw), asu(o1), xw};
    __builtin_amdgcn_raw_buffer_store_b128(rec1, rs, WS_O + (h * NSPLIT + s) * 1024 + lane * 16, 0, AUX_SC1);
    if (lane == 0) LDS_ST(lflag + 5, 1u);
    return;
  }

  const size_t kvbase = ((size_t)h * SK + (SK - WIN)) * DH * 2;
  const unsigned loff = lane * 16;
  const int rot = h >> 2;
  v4u qp = {0u, 0u, 0u, 0u};
  unsigned tag = 0u;
  if (w >= AHEAD) wg_barrier();
  for (int j = w; j < nu; j += NKV) {
    const int jj = j < NU ? j : NU - 1, ss = j < NU ? s : s + 1;
    const int unit = ((jj + h) & (NU - 1)) * NSPLIT + ((ss + jj) & 7);
    if (j >= AHEAD) lds_wait_ge(lflag + 2, j - AHEAD + 1);
    size_t uoff = kvbase + (size_t)unit * (UT * DH * 2);
    asm volatile("" : "+s"(uoff));
    unsigned long long kb, vb;
    asm volatile("s_waitcnt lgkmcnt(0)\n\ts_mov_b64 %0, %2\n\ts_mov_b64 %1, %3"
                 : "=&s"(kb), "=&s"(vb)
                 : "s"(__builtin_shufflevector(ka0, ka0, 2, 3)), "s"(__builtin_shufflevector(ka0, ka0, 4, 5)));
    const GAS unsigned char* kp = (const GAS unsigned char*)kb + uoff;
    const GAS unsigned char* vp = (const GAS unsigned char*)vb + uoff;
    v4u kk[8], vv[8];
    SB();
#pragma unroll
    for (int i = 0; i < 8; ++i) kk[i] = ld_stream((const GAS v4u*)(kp + (((i + rot) & 7) << 10) + loff));
#pragma unroll
    for (int i = 0; i < 8; ++i) vv[i] = ld_stream((const GAS v4u*)(vp + (((i + rot) & 7) << 10) + loff));
    SB();
    if (j < AHEAD) wg_barrier();
    asm volatile("s_waitcnt vmcnt(12)" ::: "memory");
    if (lane == 0) LDS_ADD(lflag + 2, 1u);
    SB();
    if (j == w) {
      lds_wait_ge(lflag, 1);
      qp = *reinterpret_cast<const v4u*>(qs + 4 * c);
      tag = LDS_LD(lflag + 1);
    }
    float sc[8];
#pragma unroll
    for (int i = 0; i < 8; ++i) {
      float d = 0.f;
#pragma unroll
      for (int k = 0; k < 4; ++k) d = dot2(kk[i][k], qp[k], d);
      sc[i] = d;
    }
#define TR_STEP(HALF, CTRL, BIT)                                  \
  _Pragma("unroll") for (int i = 0; i < (HALF); ++i) {            \
    const float lo_ = sc[i], hi_ = sc[i + (HALF)];                \
    const bool up_ = (c & (BIT)) != 0;                            \
    const float keep_ = up_ ? hi_ : lo_, send_ = up_ ? lo_ : hi_; \
    sc[i] = keep_ + dpp<CTRL>(send_);                             \
  }
    TR_STEP(4, XOR8, 8) TR_STEP(2, XOR7, 4) TR_STEP(1, XOR2, 2)
#undef TR_STEP
    const float sraw = sc[0] + dpp<XOR1>(sc[0]);
    const float sv = bf16r(bf16r(sraw) * 0.08838834764831845f);
    float mx = sv;
    mx = fmaxf(mx, dpp<XOR2>(mx));
    mx = fmaxf(mx, dpp<XOR7>(mx));
    mx = fmaxf(mx, dpp<XOR8>(mx));
    mx = swap16_max(mx);
    mx = swap32_max(mx);
    const float p = __builtin_amdgcn_exp2f((sv - mx) * LOG2E);
    float l = p;
    l += dpp<XOR2>(l);
    l += dpp<XOR7>(l);
    l += dpp<XOR8>(l);
    l = swap16_add(l, l);
    l = swap32_add(l, l);
    if (lane == 0) *reinterpret_cast<f32x2*>(wml + 2 * j) = f32x2{mx, l};
    float acc[8];
#pragma unroll
    for (int k = 0; k < 8; ++k) acc[k] = 0.f;
#define PV_STEP(I)                                                       \
  {                                                                      \
    const float pi_ = dpp<0x150 + 2 * (I)>(p);          \
    _Pragma("unroll") for (int k = 0; k < 4; ++k) {                      \
      const unsigned u_ = vv[I][k];                                      \
      acc[2 * k] = fmaf(pi_, asf(u_ << 16), acc[2 * k]);                 \
      acc[2 * k + 1] = fmaf(pi_, asf(u_ & 0xffff0000u), acc[2 * k + 1]); \
    }                                                                    \
    asm volatile("" : "+v"(acc[0]), "+v"(acc[1]), "+v"(acc[2]), "+v"(acc[3]), "+v"(acc[4]), "+v"(acc[5]), "+v"(acc[6]), "+v"(acc[7])); \
  }
    PV_STEP(0) PV_STEP(1) PV_STEP(2) PV_STEP(3) PV_STEP(4) PV_STEP(5) PV_STEP(6) PV_STEP(7)
#undef PV_STEP
    float u[4], t[2];
#pragma unroll
    for (int k = 0; k < 4; ++k) u[k] = swap32_add(acc[k], acc[k + 4]);
#pragma unroll
    for (int k = 0; k < 2; ++k) t[k] = swap16_add(u[k], u[k + 2]);
    *reinterpret_cast<f32x2*>(redo + j * DH + 8 * c + 2 * r) = f32x2{t[0], t[1]};
    LDS_ORDER();
    if (lane == 0) LDS_ADD(lflag + 3, 1u << j);
  }
  KA_WAIT();
  const __amdgpu_buffer_rsrc_t rs = __builtin_amdgcn_make_buffer_rsrc((void*)ws_, 0, WS_END, 0x00020000);
  if (w >= 2 && w <= 9) {
    const int tw = w - 2;
    const int ch = c < 10 ? c : 9;
    const GAS v4u* wop = (const GAS v4u*)(wo_ + (size_t)(h2 * DH + 16 * tw + 4 * r) * DM + c2 * CB + 8 * ch);
    lds_wait_ge(lflag + 2, nu);
    v4u wo[4];
#pragma unroll
    for (int jj = 0; jj < 4; ++jj) wo[jj] = ld_stream(wop + jj * (DM / 8));
    SB();
    const int sp = c & 7, pr = c >> 3;
    const int ooff = WS_O + (h2 * NSPLIT + sp) * 1024 + (16 * tw + 4 * r + 2 * pr) * 8;
    unsigned p01[8], p23[8];
#pragma unroll
    for (int k = 0; k < 4; ++k) {
      p01[2 * k] = __builtin_amdgcn_perm(wo[1][k], wo[0][k], 0x05040100u);
      p01[2 * k + 1] = __builtin_amdgcn_perm(wo[1][k], wo[0][k], 0x07060302u);
      p23[2 * k] = __builtin_amdgcn_perm(wo[3][k], wo[2][k], 0x05040100u);
      p23[2 * k + 1] = __builtin_amdgcn_perm(wo[3][k], wo[2][k], 0x07060302u);
    }
    asm volatile("" : "+v"(p01[0]), "+v"(p01[1]), "+v"(p01[2]), "+v"(p01[3]), "+v"(p01[4]), "+v"(p01[5]), "+v"(p01[6]), "+v"(p01[7]));
    asm volatile("" : "+v"(p23[0]), "+v"(p23[1]), "+v"(p23[2]), "+v"(p23[3]), "+v"(p23[4]), "+v"(p23[5]), "+v"(p23[6]), "+v"(p23[7]));
    lds_wait_ge(lflag + 5, 1);
    unsigned bo0 = 0u, bo1 = 0u, bx = 0u;
    int go = ooff;
    for (unsigned spins = 0; spins <= SPIN_CAP; ++spins) {
      asm volatile("" : "+v"(go));
      const v4u ra = ld_sc1(rs, go);
      const bool to = (ra[1] ^ mix(ra[2], ra[3])) == tag;
      bo0 = to ? ra[0] : bo0; bo1 = to ? ra[2] : bo1; bx = to ? ra[3] : bx;
      go = to ? OOB : go;
      if (__builtin_amdgcn_ballot_w64(go != OOB) == 0ull) break;
      __builtin_amdgcn_s_sleep(1);
    }
    const unsigned bxo = dppu<XOR8>(bx);
    const float Ms = asf(pr ? bxo : bx), Ls = asf(pr ? bx : bxo);
    float Mh = Ms;
    Mh = fmaxf(Mh, dpp<XOR1>(Mh));
    Mh = fmaxf(Mh, dpp<XOR2>(Mh));
    Mh = fmaxf(Mh, dpp<XOR7>(Mh));
    const float ws = __builtin_amdgcn_exp2f((Ms - Mh) * LOG2E);
    float Lh = ws * Ls, o0 = ws * asf(bo0), o1 = ws * asf(bo1);
    Lh += dpp<XOR1>(Lh); o0 += dpp<XOR1>(o0); o1 += dpp<XOR1>(o1);
    Lh += dpp<XOR2>(Lh); o0 += dpp<XOR2>(o0); o1 += dpp<XOR2>(o1);
    Lh += dpp<XOR7>(Lh); o0 += dpp<XOR7>(o0); o1 += dpp<XOR7>(o1);
    const float inv = __builtin_amdgcn_rcpf(Lh);
    const unsigned mine = cvtpk(o0 * inv, o1 * inv);
    const unsigned other = dppu<XOR8>(mine);
    const unsigned o01 = pr ? other : mine, o23 = pr ? mine : other;
    float g8[8];
#pragma unroll
    for (int k = 0; k < 8; ++k) g8[k] = dot2(p23[k], o23, dot2(p01[k], o01, 0.f));
    float u[4], t[2];
#pragma unroll
    for (int k = 0; k < 4; ++k) u[k] = swap32_add(g8[k], g8[k + 4]);
#pragma unroll
    for (int k = 0; k < 2; ++k) t[k] = swap16_add(u[k], u[k + 2]);
    *reinterpret_cast<f32x2*>(redw + tw * DH + 8 * c + 2 * r) = f32x2{t[0], t[1]};
    LDS_ORDER();
    unsigned old = 0u;
    if (lane == 0) old = LDS_ADD(lflag + 4, 1u);
    old = __builtin_amdgcn_readfirstlane(old);
    asm volatile("" ::: "memory");
    if (old == 7u && lane < CB / 2) {
      f32x2 o = {0.f, 0.f};
#pragma unroll
      for (int ww = 0; ww < 8; ++ww) o += *reinterpret_cast<const f32x2*>(redw + ww * DH + 2 * lane);
      const v4u rec3 = {asu(o[0]), tag, asu(o[1]), tag};
      const int cg = c2 * (CB / 16) + (lane >> 3);
      __builtin_amdgcn_raw_buffer_store_b128(rec3, rs, WS_H2 + cg * 4096 + h2 * 128 + (lane & 7) * 16, 0, AUX_SC1);
    }
  } else if (w == 10 && h2 < CB / 4) {
    const int cg = c2 * (CB / 4) + h2;
    lds_wait_ge(lflag + 4, 1);
    unsigned b0 = 0u, b1 = 0u;
    int go = WS_H2 + (cg >> 2) * 4096 + (lane & 31) * 128 + (cg & 3) * 32 + (lane >> 5) * 16;
    for (unsigned spins = 0; spins <= SPIN_CAP; ++spins) {
      asm volatile("" : "+v"(go));
      const v4u ra = ld_sc1(rs, go);
      const bool tv = tagged(ra, tag);
      b0 = tv ? ra[0] : b0; b1 = tv ? ra[2] : b1;
      go = tv ? OOB : go;
      if (__builtin_amdgcn_ballot_w64(go != OOB) == 0ull) break;
    }
    float a0 = asf(b0), a1 = asf(b1);
    a0 += dpp<XOR1>(a0); a1 += dpp<XOR1>(a1);
    a0 += dpp<XOR2>(a0); a1 += dpp<XOR2>(a1);
    a0 += dpp<XOR7>(a0); a1 += dpp<XOR7>(a1);
    a0 += dpp<XOR8>(a0); a1 += dpp<XOR8>(a1);
    a0 = swap16_add(a0, a0); a1 = swap16_add(a1, a1);
    const __amdgpu_buffer_rsrc_t ro = __builtin_amdgcn_make_buffer_rsrc((void*)out_, 0, DM * 2, 0x00020000);
    __builtin_amdgcn_raw_buffer_store_b32(cvtpk(a0, a1), ro, (lane & 31) == 0 ? 8 * cg + 4 * (lane >> 5) : OOB, 0, 0);
    if (cg == 0 && lane == 0)
      __hip_atomic_store((GAS unsigned*)(ws_ + WS_EPOCH), tag, __ATOMIC_RELAXED, __HIP_MEMORY_SCOPE_AGENT);
  }
}

}

extern "C" void kernel_launch(void* const* d_in, const int* in_sizes, int n_in, void* d_out, int out_size, void* d_ws,
                              size_t ws_size, hipStream_t stream) {
  (void)in_sizes; (void)n_in; (void)out_size; (void)ws_size;
  decode_kernel<<<dim3(NH * NSPLIT), dim3(NTHR), 0, stream>>>(
      (const unsigned short*)d_in[0], (const unsigned short*)d_in[1], (const unsigned short*)d_in[2],
      (const unsigned short*)d_in[3], (const unsigned short*)d_in[4], (unsigned short*)d_out, (unsigned char*)d_ws);
}
```
